```python
import torch

def _softmax(x, axis=-1):
    return torch.softmax(x, dim=axis)
def _gelu(x):
    return torch.nn.functional.gelu(x, approximate="tanh")
def _silu(x):
    return torch.nn.functional.silu(x)
def _sigmoid(x):
    return torch.sigmoid(x)
def _layernorm(x, axis=-1, eps=1e-5):
    m = x.mean(dim=axis, keepdim=True)
    v = x.var(dim=axis, keepdim=True, unbiased=False)
    return (x - m) / torch.sqrt(v + eps)
def _scatter_add(tgt, idx, src, axis=0):
    return tgt.clone().index_add_(axis, idx, src)
def _segment_sum(src, seg, n):
    out = torch.zeros((n,) + src.shape[1:], dtype=src.dtype)
    return out.index_add_(0, seg, src)
def _pad(x, pw):
    flat = tuple(v for pair in reversed(pw) for v in pair)
    return torch.nn.functional.pad(x, flat)

kernel_name = 'sequential_preLN__linear_kernel_hedgehog__dense_activation_gelu__prompt'

def setup_inputs():

    _w = (lambda a: (lambda t: t.to(torch.bfloat16) if t.is_floating_point() else t)(torch.as_tensor(a)))
    def _A_setup():
        B, S, D, H = 1, 2048, 512, 32
        Dh = D // H
        return {
            "x": _w((lambda *s: torch.randn(*s, dtype=torch.bfloat16) * 0.05)(B, S, D)),
            "wqkv": _w((lambda *s: torch.randn(*s, dtype=torch.bfloat16) * 0.2)(D, 3 * D)),
            "w_phi": _w((lambda *s: torch.randn(*s, dtype=torch.bfloat16) * 0.2)(H, Dh, Dh) * (Dh ** -0.5)),
            "wo": _w((lambda *s: torch.randn(*s, dtype=torch.bfloat16) * 0.05)(D, D)),
        }
    _A_in = _A_setup()

    def _B_setup():
        B, S, D, F = 1, 2048, 512, 1376
        return {
            "x": _w((lambda *s: torch.randn(*s, dtype=torch.bfloat16) * 0.05)(B, S, D)),
            "w1": _w((lambda *s: torch.randn(*s, dtype=torch.bfloat16) * 1.6)(D, F) * (D ** -0.5)),
            "b1": _w((lambda *s: torch.randn(*s, dtype=torch.bfloat16) * 0.1)(F) * 0.01),
            "w2": _w((lambda *s: torch.randn(*s, dtype=torch.bfloat16) * 0.1)(F, D) * (F ** -0.5)),
            "b2": _w((lambda *s: torch.randn(*s, dtype=torch.bfloat16) * 0.1)(D) * 0.01),
        }
    _B_in = _B_setup()

    return {
        "A_x": _A_in["x"],
        "A_wqkv": _A_in["wqkv"],
        "A_w_phi": _A_in["w_phi"],
        "A_wo": _A_in["wo"],
        "B_x": _B_in["x"],
        "B_w1": _B_in["w1"],
        "B_b1": _B_in["b1"],
        "B_w2": _B_in["w2"],
        "B_b2": _B_in["b2"],
    }

def reference(A_x, A_wqkv, A_w_phi, A_wo, B_x, B_w1, B_b1, B_w2, B_b2):
    def _A_ref(x, wqkv, w_phi, wo):
        B, S, D = x.shape
        H = 32; Dh = D // H
        qkv = (lambda x, *d: x.permute(*d))((x @ wqkv).reshape(B, S, 3, H, Dh), 2, 0, 3, 1, 4)
        q, k, v = qkv[0], qkv[1], qkv[2]
        pq = torch.einsum("bhsd,hdf->bhsf", q, w_phi)
        pk = torch.einsum("bhsd,hdf->bhsf", k, w_phi)
        phi_q = torch.concatenate([_softmax(pq, axis=-1), _softmax(-pq, axis=-1)], axis=-1)
        phi_k = torch.concatenate([_softmax(pk, axis=-1), _softmax(-pk, axis=-1)], axis=-1)
        kv = torch.einsum("bhsf,bhsd->bhfd", phi_k, v)
        z  = torch.einsum("bhsf->bhf", phi_k)
        num = torch.einsum("bhqf,bhfd->bhqd", phi_q, kv)
        den = torch.einsum("bhqf,bhf->bhq", phi_q, z)[..., None] + 1e-6
        o = num / den
        return (lambda x, *d: x.permute(*d))(o, 0, 2, 1, 3).reshape(B, S, D) @ wo

    def _B_ref(x, w1, b1, w2, b2):
        h = torch.einsum("bsd,df->bsf", x, w1) + b1
        h = _gelu(h)
        return torch.einsum("bsf,fd->bsd", h, w2) + b2

    x = A_x
    h = x + _A_ref(_layernorm(x, axis=-1), A_wqkv, A_w_phi, A_wo)
    h2 = h + _B_ref(_layernorm(h, axis=-1), B_w1, B_b1, B_w2, B_b2)
    return h2
```

```cpp
#include <hip/hip_runtime.h>
#include <hip/hip_bf16.h>
typedef unsigned short u16;
__device__ __forceinline__ float b2f(u16 v){ return __uint_as_float(((unsigned)v)<<16); }
__device__ __forceinline__ u16 f2b(float f){ unsigned u=__float_as_uint(f); return (u16)((u + 0x7FFF + ((u>>16)&1))>>16); }
constexpr int S=2048,D=512,H=32,DH=16,F=1376;
__global__ void k_ln(const u16* x, const float* xf, u16* y){
  int r=blockIdx.x; int t=threadIdx.x;
  float a = x? b2f(x[r*D+t]) : xf[r*D+t];
  float b = x? b2f(x[r*D+t+256]) : xf[r*D+t+256];
  __shared__ float sm[256];
  sm[t]=a+b; __syncthreads();
  for(int o=128;o>0;o>>=1){ if(t<o) sm[t]+=sm[t+o]; __syncthreads(); }
  float m=sm[0]/D; __syncthreads();
  sm[t]=(a-m)*(a-m)+(b-m)*(b-m); __syncthreads();
  for(int o=128;o>0;o>>=1){ if(t<o) sm[t]+=sm[t+o]; __syncthreads(); }
  float v=sm[0]/D; float rs=1.0f/sqrtf(v+1e-5f);
  y[r*D+t]=f2b((a-m)*rs); y[r*D+t+256]=f2b((b-m)*rs);
}
__global__ void k_gemm(const u16* A,const u16* B,int N,int K,int mode,const u16* bias,const u16* res,const float* resf,u16* out,float* outf){
  int n=blockIdx.x*64+threadIdx.x; int m0=blockIdx.y*8;
  if(n>=N) return;
  float acc[8]={0,0,0,0,0,0,0,0};
  for(int k=0;k<K;k++){ float b=b2f(B[k*N+n]);
    for(int i=0;i<8;i++) acc[i]+=b2f(A[(m0+i)*K+k])*b; }
  for(int i=0;i<8;i++){ int m=m0+i; float v=acc[i];
    if(mode==0) out[m*N+n]=f2b(v);
    else if(mode==1) outf[m*N+n]=b2f(res[m*N+n])+v;
    else if(mode==2){ v+=b2f(bias[n]); float u=0.7978845608f*(v+0.044715f*v*v*v); out[m*N+n]=f2b(0.5f*v*(1.f+tanhf(u))); }
    else out[m*N+n]=f2b(resf[m*N+n]+v+b2f(bias[n])); }
}
__global__ void k_phi(const u16* qkv,const u16* wphi,float* phi){
  int id=blockIdx.x*256+threadIdx.x; int which=id/(S*H); int s=(id/H)%S; int h=id%H;
  float p[16];
  for(int f=0;f<16;f++){ float a=0; for(int d=0;d<16;d++) a+=b2f(qkv[s*3*D+which*D+h*16+d])*b2f(wphi[h*256+d*16+f]); p[f]=a; }
  float mx=-1e30f,mn=1e30f; for(int f=0;f<16;f++){mx=fmaxf(mx,p[f]); mn=fminf(mn,p[f]);}
  float s1=0,s2=0; for(int f=0;f<16;f++){ s1+=expf(p[f]-mx); s2+=expf(mn-p[f]); }
  for(int f=0;f<16;f++){ phi[(size_t)id*32+f]=expf(p[f]-mx)/s1; phi[(size_t)id*32+16+f]=expf(mn-p[f])/s2; }
}
__global__ void k_kv(const float* phik,const u16* qkv,float* kv){
  int h=blockIdx.x; int t=threadIdx.x; if(t>=32*17) return; int f=t/17,d=t%17;
  float a=0; for(int s=0;s<S;s++){ float v = d<16? b2f(qkv[s*3*D+2*D+h*16+d]) : 1.f; a+=phik[(s*H+h)*32+f]*v; }
  kv[h*544+t]=a;
}
__global__ void k_o(const float* phiq,const float* kv,u16* o){
  int id=blockIdx.x*256+threadIdx.x; int s=id/D; int h=(id%D)/16; int d=id%16;
  float num=0,den=0; for(int f=0;f<32;f++){ float p=phiq[(s*H+h)*32+f]; num+=p*kv[h*544+f*17+d]; den+=p*kv[h*544+f*17+16]; }
  o[id]=f2b(num/(den+1e-6f));
}
extern "C" void kernel_launch(void* const* d_in, const int* in_sizes, int n_in, void* d_out, int out_size, void* d_ws, size_t ws_size, hipStream_t stream) {
  const u16* x=(const u16*)d_in[0]; const u16* wqkv=(const u16*)d_in[1]; const u16* wphi=(const u16*)d_in[2]; const u16* wo=(const u16*)d_in[3];
  const u16* w1=(const u16*)d_in[5]; const u16* b1=(const u16*)d_in[6]; const u16* w2=(const u16*)d_in[7]; const u16* b2=(const u16*)d_in[8];
  char* ws=(char*)d_ws;
  u16* xn=(u16*)ws; u16* qkv=(u16*)(ws+(4<<20)); float* phi=(float*)(ws+(16<<20)); float* kv=(float*)(ws+(40<<20)); u16* o=(u16*)(ws+(44<<20));
  float* hf=(float*)(ws+(48<<20)); u16* hn=(u16*)(ws+(56<<20)); u16* h1=(u16*)(ws+(60<<20));
  k_ln<<<S,256,0,stream>>>(x,nullptr,xn);
  k_gemm<<<dim3(1536/64,S/8),64,0,stream>>>(xn,wqkv,1536,512,0,nullptr,nullptr,nullptr,qkv,nullptr);
  k_phi<<<2*S*H/256,256,0,stream>>>(qkv,wphi,phi);
  k_kv<<<H,576,0,stream>>>(phi+(size_t)S*H*32,qkv,kv);
  k_o<<<S*D/256,256,0,stream>>>(phi,kv,o);
  k_gemm<<<dim3(512/64,S/8),64,0,stream>>>(o,wo,512,512,1,nullptr,x,nullptr,nullptr,hf);
  k_ln<<<S,256,0,stream>>>(nullptr,hf,hn);
  k_gemm<<<dim3((F+63)/64,S/8),64,0,stream>>>(hn,w1,F,512,2,b1,nullptr,nullptr,h1,nullptr);
  k_gemm<<<dim3(512/64,S/8),64,0,stream>>>(h1,w2,512,F,3,b2,nullptr,hf,(u16*)d_out,nullptr);
}
```

```cpp
#include <hip/hip_runtime.h>
#include <hip/hip_bf16.h>
#pragma clang diagnostic ignored "-Wint-to-pointer-cast"

typedef unsigned short u16;
typedef unsigned long long u64;
typedef __attribute__((ext_vector_type(8))) __bf16 bf16x8;
typedef __attribute__((ext_vector_type(2))) __bf16 bf16x2;
typedef __attribute__((ext_vector_type(2))) float f32x2;
typedef __attribute__((ext_vector_type(4))) float f32x4;
typedef __attribute__((ext_vector_type(16))) float f32x16;
typedef __attribute__((ext_vector_type(4))) unsigned u32x4;
typedef __attribute__((ext_vector_type(2))) unsigned u32x2;
typedef __attribute__((ext_vector_type(4))) short s16x4;
#define LAS __attribute__((address_space(3)))
#define DEV __device__ __forceinline__

constexpr unsigned WS_EPOCH = 0;
constexpr unsigned WS_FLAG  = 4096;
constexpr unsigned WS_WO    = WS_FLAG + 8 * 256 * 128;
constexpr unsigned WS_W2    = WS_WO + 8 * 32 * 2 * 1024;
constexpr unsigned WS_W1    = WS_W2 + 8 * 88 * 2 * 1024;
constexpr unsigned WS_KV    = WS_W1 + 86 * 16 * 1024;
constexpr unsigned WS_O     = WS_KV + 32 * 32 * 1152;
constexpr unsigned WS_H     = WS_O + 32 * 64 * 1024;
constexpr unsigned WS_HS    = WS_H + 32 * 64 * 1024;
constexpr unsigned WS_H1    = WS_HS + 32 * 4096;
constexpr unsigned WS_END   = WS_H1 + 32 * 2 * 88 * 1024;
constexpr unsigned WS_DBG   = 32u << 20;
constexpr unsigned OOB      = 0x7ff00000u;

constexpr unsigned L_X    = 0;
constexpr unsigned L_RING = 65536;
constexpr unsigned L_T0   = 131072;
constexpr unsigned L_T1   = L_T0 + 8192;
constexpr unsigned L_PHI  = L_T1 + 8192;
constexpr unsigned L_ST   = L_PHI + 4096;
constexpr unsigned L_MR   = L_ST + 4096;
constexpr unsigned L_EP   = L_T0;
constexpr unsigned L_H    = 0;
constexpr unsigned L_HS   = 65536;
constexpr unsigned L_R    = 73728;
constexpr unsigned L_END  = 160 * 1024;
static_assert(L_MR + 2048 <= L_END && L_R + 64 * 1152 <= L_PHI, "lds");

constexpr unsigned SPIN_CAP = 1u << 16;

DEV unsigned pk(float a, float b) { bf16x2 v = {(__bf16)a, (__bf16)b}; return __builtin_bit_cast(unsigned, v); }
DEV float blo(unsigned v) { return __builtin_bit_cast(float, v << 16); }
DEV float bhi(unsigned v) { return __builtin_bit_cast(float, v & 0xffff0000u); }
DEV f32x16 mfma32(u32x4 a, u32x4 b, f32x16 c) { return __builtin_amdgcn_mfma_f32_32x32x16_bf16(__builtin_bit_cast(bf16x8, a), __builtin_bit_cast(bf16x8, b), c, 0, 0, 0); }
DEV f32x4 mfma16(u32x4 a, u32x4 b, f32x4 c) { return __builtin_amdgcn_mfma_f32_16x16x32_bf16(__builtin_bit_cast(bf16x8, a), __builtin_bit_cast(bf16x8, b), c, 0, 0, 0); }
DEV u32x2 trrd(unsigned a) { return __builtin_bit_cast(u32x2, __builtin_amdgcn_ds_read_tr16_b64_v4i16((LAS s16x4*)a)); }
DEV u32x4 trfrag(unsigned a0, unsigned a1) { u32x2 x = trrd(a0), y = trrd(a1); return u32x4{x.x, x.y, y.x, y.y}; }
DEV u32x4 lrd(unsigned a) { return *(LAS u32x4*)a; }
DEV void lwr(unsigned a, u32x4 v) { *(LAS u32x4*)a = v; }
DEV float dot2(unsigned a, unsigned b, float c) { return __builtin_amdgcn_fdot2_f32_bf16(__builtin_bit_cast(bf16x2, a), __builtin_bit_cast(bf16x2, b), c, false); }
DEV u32x4 srd(const void* p, unsigned bytes) { u64 a = (u64)p; return u32x4{(unsigned)a, (unsigned)(a >> 32) & 0xffffu, bytes, 0x00020000u}; }
DEV void glds(u32x4 rs, unsigned lds, unsigned voff, unsigned soff) {
  asm volatile("s_mov_b32 m0, %0\n\ts_nop 0\n\tbuffer_load_dwordx4 %1, %2, %3 offen lds" ::"s"(lds), "v"(voff), "s"(rs), "s"(soff) : "memory");
}
DEV void glds_dev(u32x4 rs, unsigned lds, unsigned voff, unsigned soff) {
  asm volatile("s_mov_b32 m0, %0\n\ts_nop 0\n\tbuffer_load_dwordx4 %1, %2, %3 offen sc1 lds" ::"s"(lds), "v"(voff), "s"(rs), "s"(soff) : "memory");
}
#define VMW(n) asm volatile("s_waitcnt vmcnt(" #n ")" ::: "memory")
#define BAR() asm volatile("s_waitcnt lgkmcnt(0)\n\ts_barrier" ::: "memory")
#define FENCE() do { asm volatile("" ::: "memory"); __builtin_amdgcn_sched_barrier(0); } while (0)
typedef __amdgpu_buffer_rsrc_t rsrc_t;
DEV u32x4 ld16(rsrc_t r, unsigned v, unsigned s) { return __builtin_amdgcn_raw_buffer_load_b128(r, v, s, 16); }
DEV void st16(rsrc_t r, u32x4 d, unsigned v, unsigned s) { __builtin_amdgcn_raw_buffer_store_b128(d, r, v, s, 16); }
DEV void swap16(unsigned& a, unsigned& b) { auto r = __builtin_amdgcn_permlane16_swap(a, b, false, false); a = r[0]; b = r[1]; }
DEV float xsum16(float v) { unsigned a = __builtin_bit_cast(unsigned, v); auto r = __builtin_amdgcn_permlane16_swap(a, a, false, false); return __builtin_bit_cast(float, (unsigned)r[0]) + __builtin_bit_cast(float, (unsigned)r[1]); }
DEV float xsum32(float v) { unsigned a = __builtin_bit_cast(unsigned, v); auto r = __builtin_amdgcn_permlane32_swap(a, a, false, false); return __builtin_bit_cast(float, (unsigned)r[0]) + __builtin_bit_cast(float, (unsigned)r[1]); }
DEV float xsum8(float v) { return v + __builtin_bit_cast(float, __builtin_amdgcn_update_dpp(0, __builtin_bit_cast(int, v), 0x128, 0xf, 0xf, true)); }

DEV void wait_flags(rsrc_t ws, unsigned voff, unsigned soff, u32x2 tag) {
  unsigned sp = 0;
#pragma nounroll
  for (;;) {
    const u32x2 f = __builtin_amdgcn_raw_buffer_load_b64(ws, voff, soff, 16);
    if (__builtin_amdgcn_ballot_w64(((f.x ^ tag.x) | (f.y ^ tag.y)) != 0u) == 0ull || ++sp >= SPIN_CAP) break;
    __builtin_amdgcn_s_sleep(1);
  }
  FENCE();
}
DEV void set_flag(rsrc_t ws, unsigned off, u32x2 tag, int tid) {
  if (tid == 0) __builtin_amdgcn_raw_buffer_store_b64(tag, ws, off, 0, 16);
  FENCE();
}

DEV void softmax16(const f32x16& p, float c, unsigned (&o)[8]) {
  float m = fmaxf(fmaxf(p[0], p[1]), p[2]);
#pragma unroll
  for (int i = 3; i < 15; i += 2) m = fmaxf(fmaxf(m, p[i]), p[i + 1]);
  m = fmaxf(m, p[15]);
  float nb = -m * c, e[16], s = 0.f;
#pragma unroll
  for (int i = 0; i < 16; ++i) { e[i] = __builtin_amdgcn_exp2f(fmaf(p[i], c, nb)); s += e[i]; }
  float inv = __builtin_amdgcn_rcpf(s);
#pragma unroll
  for (int i = 0; i < 8; ++i) o[i] = pk(e[2 * i] * inv, e[2 * i + 1] * inv);
}

#define TS(i)

__global__ __launch_bounds__(256, 1) void hh_block(const u16* __restrict__ x, const u16* __restrict__ wqkv, const u16* __restrict__ wphi,
                                                   const u16* __restrict__ wo, const u16* __restrict__ w1, const u16* __restrict__ b1,
                                                   const u16* __restrict__ w2, const u16* __restrict__ b2, u16* __restrict__ out, char* __restrict__ wsp) {
  const int tid = threadIdx.x, l = tid & 63;
  const unsigned w = __builtin_amdgcn_readfirstlane(tid >> 6);
  const unsigned b = blockIdx.x, p = (b & 7) * 4 + (b >> 6), g = (b >> 3) & 7, wg = p * 8 + g;
  TS(0);
  const rsrc_t ws = __builtin_amdgcn_make_buffer_rsrc(wsp, 0, WS_END, 0x00020000);
  const u32x4 sx = srd(x, 2048 * 512 * 2), sw = srd(wqkv, 512 * 1536 * 2);
  const unsigned t0 = g < 6 ? 11 * g : 66 + 10 * (g - 6), nt = g < 6 ? 11 : 10;

  const unsigned r8 = l >> 3, s8 = l & 7;
  unsigned vX[2], vW[2];
#pragma unroll
  for (int e = 0; e < 2; ++e) {
    vX[e] = (64 * p + 16 * w + 8 * e + r8) * 1024 + ((s8 ^ (4 * e + (r8 >> 1))) << 4);
    vW[e] = r8 * 3072 + g * 128 + (((((s8 >> 1) ^ ((r8 & 2) | e)) << 1) | (s8 & 1)) << 4);
  }
  auto stage = [&](int S) {
    const unsigned slot = L_RING + (S & 3) * 16384;
    if (S < 8) {
      if (S < 4) {
#pragma unroll
        for (int e = 0; e < 2; ++e) glds(sx, L_X + (2 * S) * 8192 + (2 * w + e) * 1024, vX[e], (2 * S) * 128);
      }
#pragma unroll
      for (int e = 0; e < 2; ++e)
#pragma unroll
        for (int sg = 0; sg < 2; ++sg) glds(sw, slot + sg * 8192 + (2 * w + e) * 1024, vW[sg], (S * 64 + 16 * w + 8 * e) * 3072 + (1 + sg) * 1024);
      if (S < 4) {
#pragma unroll
        for (int e = 0; e < 2; ++e) glds(sx, L_X + (2 * S + 1) * 8192 + (2 * w + e) * 1024, vX[e], (2 * S + 1) * 128);
      }
    } else {
#pragma unroll
      for (int e = 0; e < 4; ++e) glds(sw, slot + (4 * w + e) * 1024, vW[e & 1], ((S - 8) * 128 + 32 * w + 8 * e) * 3072);
    }
  };
  stage(0);
  const unsigned tks = w == 0 ? p : p + 32 * (w - 1);
  {
    const u32x4 st = w == 0 ? srd(wo, 512 * 512 * 2) : srd(w2, 1376 * 512 * 2);
#pragma unroll
    for (int e = 0; e < 2; ++e) glds(st, L_T0 + w * 2048 + e * 1024, r8 * 1024 + g * 128 + s8 * 16, ((tks < 86 ? tks : 85) * 16 + 8 * e) * 1024);
    const u32x4 s1 = srd(w1, 512 * 1376 * 2);
    const unsigned v1 = (l & 31) < 2 * nt ? (l >> 5) * 2752 + t0 * 32 + (l & 31) * 16 : OOB;
#pragma unroll
    for (int e = 0; e < 2; ++e) glds(s1, L_T1 + (2 * w + e) * 1024, v1, (16 * p + 4 * w + 2 * e) * 2752);
    glds(srd(wphi, 32 * 256 * 2), L_PHI + w * 1024, l < 32 ? l * 16 : OOB, (4 * g + w) * 512);
  }
  stage(1);
  stage(2);
  const u64 ep = *(const __attribute__((address_space(4))) u64*)(wsp + WS_EPOCH) + 1;
  const u32x2 tag = {(unsigned)ep, (unsigned)(ep >> 32)};

  const unsigned a4 = l >> 4, q4 = (l >> 2) & 3, p4 = l & 3, h = l >> 5, t5 = l & 31, t4 = l & 15;
  const unsigned aW = (a4 & 1) * 8192 + h * 1024 + q4 * 128 + ((w ^ ((q4 & 2) | (a4 & 1))) << 5) + ((((p4 & 1) << 1) | (p4 >> 1)) << 3);
  const unsigned aX = t5 * 128 + ((h ^ ((t5 >> 1) & 7)) << 4);
  const u32x4 ones = {0x3f803f80u, 0x3f803f80u, 0x3f803f80u, 0x3f803f80u};
  f32x16 acc[2] = {}, cs = {};
  float su[2] = {0.f, 0.f}, sq[2] = {0.f, 0.f};
  float mean5[2], rstd5[2], mean4[4];
#pragma unroll
  for (int S = 0; S < 8; ++S) {
    if (S == 0) VMW(21); else if (S == 1) VMW(16); else if (S == 2) VMW(12); else VMW(8);
    BAR();
    stage(S + 3);
    const unsigned slot = L_RING + (S & 3) * 16384;
#pragma unroll
    for (int ks = 0; ks < 4; ++ks) {
      u32x4 wf = trfrag(slot + aW + ks * 2048, slot + aW + ks * 2048 + 512);
#pragma unroll
      for (int tt = 0; tt < 2; ++tt) acc[tt] = mfma32(wf, lrd(L_X + S * 8192 + tt * 4096 + (aX ^ (ks * 32))), acc[tt]);
      cs = mfma32(wf, ones, cs);
    }
    if (S < 4) {
#pragma unroll
      for (int c = 0; c < 2; ++c)
#pragma unroll
        for (int tt = 0; tt < 2; ++tt) {
          u32x4 v = lrd(L_X + (2 * S + c) * 8192 + tt * 4096 + (aX ^ (w * 32)));
#pragma unroll
          for (int i = 0; i < 4; ++i) { su[tt] = dot2(v[i], 0x3f803f80u, su[tt]); sq[tt] = dot2(v[i], v[i], sq[tt]); }
        }
    }
    if (S == 1) {
#pragma unroll
      for (int ct = 0; ct < 2; ++ct) {
        const unsigned a = L_T0 + w * 2048 + (8 * h + q4) * 128 + ct * 64 + (a4 & 1) * 32 + p4 * 8;
        const unsigned dst = w == 0 ? WS_WO + ((g * 32 + tks) * 2 + ct) * 1024 : WS_W2 + ((g * 88 + tks) * 2 + ct) * 1024;
        st16(ws, trfrag(a, a + 512), tks < 86 ? l * 16 : OOB, dst);
      }
#pragma unroll
      for (int e = 0; e < 2; ++e) {
        const unsigned tl = 2 * (w + 4 * e) + h;
        const unsigned a = L_T1 + (8 * (a4 & 1) + q4) * 512 + tl * 32 + p4 * 8;
        st16(ws, trfrag(a, a + 2048), tl < nt ? (t0 + tl) * 16384 + t5 * 16 : OOB, WS_W1 + (p >> 1) * 1024 + (p & 1) * 512);
      }
    }
    if (S == 3) {
#pragma unroll
      for (int tt = 0; tt < 2; ++tt) *(LAS f32x2*)(L_ST + ((w * 2 + h) * 64 + tt * 32 + t5) * 8) = f32x2{su[tt], sq[tt]};
    }
    if (S == 4) {
      f32x2 a = {0.f, 0.f};
#pragma unroll
      for (int i = 0; i < 8; ++i) a += *(LAS f32x2*)(L_ST + (i * 64 + l) * 8);
      const float mean = a.x * (1.f / 512), var = fmaxf(a.y * (1.f / 512) - mean * mean, 0.f);
      *(LAS f32x2*)(L_MR + (w * 64 + l) * 8) = f32x2{mean, __builtin_amdgcn_rsqf(var + 1e-5f)};
    }
    if (S == 5) {
#pragma unroll
      for (int tt = 0; tt < 2; ++tt) { f32x2 a = *(LAS f32x2*)(L_MR + (w * 64 + tt * 32 + t5) * 8); mean5[tt] = a.x; rstd5[tt] = a.y; }
#pragma unroll
      for (int m = 0; m < 4; ++m) mean4[m] = *(LAS float*)(L_MR + (w * 64 + m * 16 + t4) * 8);
    }
  }
  TS(1);
  u32x4 pf;
  {
    const unsigned a = L_PHI + w * 1024 + (8 * h + q4) * 32 + (2 * (a4 & 1) + (p4 >> 1)) * 8;
    pf = trfrag(a, a + 128) ^ ((l & 4) ? 0x80008000u : 0u);
  }
  constexpr float LOG2E = 1.4426950408889634f;
  {
    f32x4 kv0 = {}, kv1 = {}, z0 = {}, z1 = {};
    const unsigned ePH = L_EP + w * 4096, eV = ePH + 2560;
#pragma unroll
    for (int tt = 0; tt < 2; ++tt) {
      float kk[16];
#pragma unroll
      for (int i = 0; i < 16; ++i) kk[i] = fmaf(-mean5[tt], cs[i], acc[tt][i]);
      f32x16 pp = mfma32(pf, u32x4{pk(kk[0], kk[1]), pk(kk[2], kk[3]), pk(kk[4], kk[5]), pk(kk[6], kk[7])}, f32x16{});
      const float rs = rstd5[tt];
      lwr(eV + t5 * 48 + h * 16, u32x4{pk(kk[8] * rs, kk[9] * rs), pk(kk[10] * rs, kk[11] * rs), pk(kk[12] * rs, kk[13] * rs), pk(kk[14] * rs, kk[15] * rs)});
      unsigned ph[8];
      softmax16(pp, rs * LOG2E, ph);
      lwr(ePH + t5 * 80 + h * 32, u32x4{ph[0], ph[1], ph[2], ph[3]});
      lwr(ePH + t5 * 80 + h * 32 + 16, u32x4{ph[4], ph[5], ph[6], ph[7]});
      const unsigned aP = ePH + (8 * a4 + q4) * 80 + p4 * 16, aV = eV + (8 * a4 + q4) * 48 + p4 * 8;
      u32x4 f0 = trfrag(aP, aP + 320), f1 = trfrag(aP + 8, aP + 328), fv = trfrag(aV, aV + 192);
      kv0 = mfma16(f0, fv, kv0); kv1 = mfma16(f1, fv, kv1);
      z0 = mfma16(f0, ones, z0); z1 = mfma16(f1, ones, z1);
    }
    VMW(0);
    BAR();
    stage(11);
    const unsigned dst = WS_KV + ((4 * g + w) * 32 + p) * 1152;
    st16(ws, u32x4{pk(kv0[0], kv0[1]), pk(kv0[2], kv0[3]), pk(kv1[0], kv1[1]), pk(kv1[2], kv1[3])}, l * 16, dst);
    st16(ws, u32x4{pk(z0[0], z0[1]), pk(z0[2], z0[3]), pk(z1[0], z1[1]), pk(z1[2], z1[3])}, t4 == 0 ? 1024 + a4 * 16 : OOB, dst);
    FENCE();
  }
  TS(2);
  u32x4 pq[4];
  u32x4 xr[2];
  {
    f32x4 aq[4] = {}, cq = {};
    const unsigned aWq = a4 * 1024 + q4 * 128 + ((w ^ ((q4 & 2) | (a4 & 1))) << 5) + p4 * 8;
    const unsigned aXq = t4 * 128 + ((a4 ^ (t4 >> 1)) << 4);
#pragma unroll
    for (int S = 8; S < 12; ++S) {
      if (S == 10) { VMW(0); BAR(); set_flag(ws, WS_FLAG + (1 * 256 + wg) * 128, tag, tid); TS(3); }
      const unsigned slot = L_RING + (S & 3) * 16384;
#pragma unroll
      for (int ks = 0; ks < 4; ++ks) {
        const int K2 = 4 * (S - 8) + ks;
        u32x4 wf = trfrag(slot + aWq + ks * 4096, slot + aWq + ks * 4096 + 512);
#pragma unroll
        for (int m = 0; m < 4; ++m) aq[m] = mfma16(wf, lrd(L_X + (K2 >> 1) * 8192 + m * 2048 + (aXq ^ ((K2 & 1) * 64))), aq[m]);
        cq = mfma16(wf, ones, cq);
      }
    }
#pragma unroll
    for (int tt = 0; tt < 2; ++tt) {
      const unsigned row = 32 * tt + 8 * w + s8;
      xr[tt] = lrd(L_X + g * 8192 + row * 128 + ((r8 ^ ((row >> 1) & 7)) << 4));
      unsigned qa[2][2];
#pragma unroll
      for (int e = 0; e < 2; ++e) {
        const f32x4 a = aq[2 * tt + e]; const float mu = mean4[2 * tt + e];
        qa[e][0] = pk(fmaf(-mu, cq[0], a[0]), fmaf(-mu, cq[1], a[1]));
        qa[e][1] = pk(fmaf(-mu, cq[2], a[2]), fmaf(-mu, cq[3], a[3]));
      }
      swap16(qa[0][0], qa[1][0]); swap16(qa[0][1], qa[1][1]);
      f32x16 pp = mfma32(pf, u32x4{qa[0][0], qa[0][1], qa[1][0], qa[1][1]}, f32x16{});
      unsigned ph[8];
      softmax16(pp, rstd5[tt] * LOG2E, ph);
#pragma unroll
      for (int i = 0; i < 4; ++i) swap16(ph[i], ph[4 + i]);
      pq[2 * tt] = u32x4{ph[0], ph[1], ph[2], ph[3]};
      pq[2 * tt + 1] = u32x4{ph[4], ph[5], ph[6], ph[7]};
    }
  }
  TS(4);
  if (w == 0) wait_flags(ws, t5 * 1024, WS_FLAG + (1 * 256 + g) * 128, tag);
  BAR();
  TS(5);
  {
    const unsigned src = WS_KV + (4 * g + w) * 32 * 1152;
    u32x4 zf[2], kp[32];
#pragma unroll
    for (int i = 0; i < 2; ++i) zf[i] = ld16(ws, t4 * 1152 + 1024 + a4 * 16, src + i * 16 * 1152);
#pragma unroll
    for (int i = 0; i < 32; ++i) kp[i] = ld16(ws, l * 16, src + i * 1152);
    FENCE();
    float inv[4];
#pragma unroll
    for (int m = 0; m < 4; ++m) {
      f32x4 d = mfma16(zf[1], pq[m], mfma16(zf[0], pq[m], f32x4{}));
      inv[m] = __builtin_amdgcn_rcpf(xsum32(xsum16((d[0] + d[1]) + (d[2] + d[3]))) + 1e-6f);
    }
    const unsigned one = (a4 == q4) ? ((p4 & 1) ? 0x3f800000u : 0x00003f80u) : 0u;
    const unsigned e0 = (p4 & 2) ? 0u : one, e1 = (p4 & 2) ? one : 0u;
    const u32x4 sel0 = {e0, e1, 0u, 0u}, sel1 = {0u, 0u, e0, e1};
    f32x4 k0 = {}, k1 = {};
#pragma unroll
    for (int i = 0; i < 32; ++i) { k0 = mfma16(sel0, kp[i], k0); k1 = mfma16(sel1, kp[i], k1); }
    const u32x4 kf = {pk(k0[0], k0[1]), pk(k0[2], k0[3]), pk(k1[0], k1[1]), pk(k1[2], k1[3])};
#pragma unroll
    for (int tt = 0; tt < 2; ++tt) {
      unsigned oa[2][2];
#pragma unroll
      for (int e = 0; e < 2; ++e) {
        const f32x4 n = mfma16(kf, pq[2 * tt + e], f32x4{}); const float iv = inv[2 * tt + e];
        oa[e][0] = pk(n[0] * iv, n[1] * iv); oa[e][1] = pk(n[2] * iv, n[3] * iv);
      }
      swap16(oa[0][0], oa[1][0]); swap16(oa[0][1], oa[1][1]);
      st16(ws, u32x4{oa[0][0], oa[0][1], oa[1][0], oa[1][1]}, l * 16, WS_O + ((p * 32 + 4 * g + w) * 2 + tt) * 1024);
    }
  }
  TS(6);
  VMW(0);
  BAR();
  set_flag(ws, WS_FLAG + (2 * 256 + wg) * 128, tag, tid);
  TS(7);
  u32x4 wof[16], w1f[3][16], w2f[44];
  unsigned sW1[3], vL[3];
  {
    const unsigned tb = t0 + ((nt == 10 && w == 3) ? 8 : 3 * w);
    const bool ok = w < (nt == 10 ? 2 : 3);
    sW1[0] = tb; sW1[1] = tb + 1; sW1[2] = ok ? tb + 2 : tb;
    vL[0] = vL[1] = l * 16; vL[2] = ok ? l * 16 : OOB;
  }
  const unsigned vT = w == 3 ? OOB : l * 16;
#pragma unroll
  for (int i = 0; i < 16; ++i) wof[i] = ld16(ws, l * 16, WS_WO + (g * 64 + w * 16 + i) * 1024);
#pragma unroll
  for (int i = 0; i < 16; ++i) w1f[0][i] = ld16(ws, l * 16, WS_W1 + sW1[0] * 16384 + i * 1024);
  FENCE();
  const unsigned vF = (l & 1) * 128, sF = WS_FLAG + (p * 8 + 2 * w) * 128;
  wait_flags(ws, vF, sF + 2 * 256 * 128, tag);
  TS(8);
  f32x16 ac[2][2] = {};
  unsigned hb[2][4];
  const unsigned aRr = L_R + r8 * 1152 + (8 * w + s8) * 16;
  {
    u32x4 of[16];
#pragma unroll
    for (int i = 0; i < 16; ++i) of[i] = ld16(ws, l * 16, WS_O + (p * 64 + w * 16 + i) * 1024);
    FENCE();
#pragma unroll
    for (int ks = 0; ks < 8; ++ks)
#pragma unroll
      for (int tt = 0; tt < 2; ++tt)
#pragma unroll
        for (int ct = 0; ct < 2; ++ct) ac[tt][ct] = mfma32(wof[2 * ks + ct], of[2 * ks + tt], ac[tt][ct]);
#pragma unroll
    for (int u = 0; u < 4; ++u)
#pragma unroll
      for (int q = 0; q < 4; ++q) {
        const f32x16& a = ac[u >> 1][u & 1];
        *(LAS f32x4*)(L_R + ((w * 4 + u) * 4 + q) * 1152 + l * 16) = f32x4{a[4 * q], a[4 * q + 1], a[4 * q + 2], a[4 * q + 3]};
      }
    BAR();
    TS(9);
    if (b == 0 && tid == 0) __builtin_amdgcn_raw_buffer_store_b64(tag, ws, WS_EPOCH, 0, 16);
    f32x2 stt;
#pragma unroll
    for (int tt = 0; tt < 2; ++tt) {
      f32x4 lo = {blo(xr[tt][0]), bhi(xr[tt][0]), blo(xr[tt][1]), bhi(xr[tt][1])}, hi = {blo(xr[tt][2]), bhi(xr[tt][2]), blo(xr[tt][3]), bhi(xr[tt][3])};
#pragma unroll
      for (int s = 0; s < 4; ++s) {
        lo += *(LAS f32x4*)(aRr + (s * 16 + tt * 8) * 1152);
        hi += *(LAS f32x4*)(aRr + (s * 16 + tt * 8) * 1152 + 512);
      }
      hb[tt][0] = pk(lo[0], lo[1]); hb[tt][1] = pk(lo[2], lo[3]); hb[tt][2] = pk(hi[0], hi[1]); hb[tt][3] = pk(hi[2], hi[3]);
      st16(ws, u32x4{hb[tt][0], hb[tt][1], hb[tt][2], hb[tt][3]}, (r8 >> 2) * 1024 + ((r8 & 3) * 16 + s8) * 16,
           WS_H + ((p * 4 + 2 * tt + (w >> 1)) * 16 + 2 * g) * 1024 + (w & 1) * 128);
      float a = 0.f, c = 0.f;
#pragma unroll
      for (int i = 0; i < 4; ++i) { a = dot2(hb[tt][i], 0x3f803f80u, a); c = dot2(hb[tt][i], hb[tt][i], c); }
      a = xsum32(xsum16(xsum8(a))); c = xsum32(xsum16(xsum8(c)));
      if (tt == 0 || r8 == 1) stt = f32x2{a, c};
    }
    __builtin_amdgcn_raw_buffer_store_b64(__builtin_bit_cast(u32x2, stt), ws, l < 16 ? l * 8 : OOB, WS_HS + wg * 512 + w * 128, 16);
  }
  TS(10);
  VMW(0);
  BAR();
  set_flag(ws, WS_FLAG + (3 * 256 + wg) * 128, tag, tid);
  TS(11);
  u32x2 b1r[3];
  {
    const rsrc_t rb1 = __builtin_amdgcn_make_buffer_rsrc((void*)b1, 0, 1376 * 2, 0x00020000);
#pragma unroll
    for (int t = 0; t < 3; ++t) b1r[t] = __builtin_amdgcn_raw_buffer_load_b64(rb1, a4 * 8, sW1[t] * 32, 0);
  }
#pragma unroll
  for (int t = 1; t < 3; ++t)
#pragma unroll
    for (int i = 0; i < 16; ++i) w1f[t][i] = ld16(ws, vL[t], WS_W1 + sW1[t] * 16384 + i * 1024);
  FENCE();
  wait_flags(ws, vF, sF + 3 * 256 * 128, tag);
  TS(12);
  {
    const u32x4 sws = srd(wsp, WS_END);
    glds_dev(sws, L_HS + w * 1024, l * 16, WS_HS + p * 4096 + w * 1024);
#pragma unroll
    for (int m = 0; m < 4; ++m)
#pragma unroll
      for (int e = 0; e < 4; ++e) glds_dev(sws, L_H + (m * 16 + 4 * w + e) * 1024, l * 16, WS_H + (p * 64 + m * 16 + 4 * w + e) * 1024);
    f32x4 c1[3] = {};
#pragma unroll
    for (int ks = 0; ks < 16; ++ks)
#pragma unroll
      for (int t = 0; t < 3; ++t) c1[t] = mfma16(w1f[t][ks], ones, c1[t]);
    asm volatile("" : "+v"(c1[0]), "+v"(c1[1]), "+v"(c1[2]));
    VMW(0);
    BAR();
    TS(13);
#pragma unroll
    for (int i = 0; i < 44; ++i) w2f[i] = ld16(ws, i >= 40 ? vT : l * 16, WS_W2 + (g * 176 + w * 44 + i) * 1024);
    FENCE();
    {
      f32x2 a = {0.f, 0.f};
      const unsigned sl = ((l >> 3) & 3) * 128 + (l >> 5) * 64 + (l & 7) * 8;
#pragma unroll
      for (int i = 0; i < 8; ++i) a += *(LAS f32x2*)(L_HS + i * 512 + sl);
      const float mean = a.x * (1.f / 512), var = fmaxf(a.y * (1.f / 512) - mean * mean, 0.f);
      const float rs = __builtin_amdgcn_rsqf(var + 1e-5f);
      *(LAS f32x2*)(L_MR + (w * 64 + l) * 8) = f32x2{-mean * rs, rs};
    }
    constexpr float K1 = -2.f * 0.7978845608028654f * LOG2E, K2 = K1 * 0.044715f;
#pragma unroll
    for (int tt = 0; tt < 2; ++tt) {
      unsigned hq[2][3][2];
#pragma unroll
      for (int e = 0; e < 2; ++e) {
        const int m = 2 * tt + e;
        f32x4 a[3] = {};
#pragma unroll
        for (int ks = 0; ks < 16; ++ks) {
          const u32x4 hf = lrd(L_H + (m * 16 + ks) * 1024 + l * 16);
#pragma unroll
          for (int t = 0; t < 3; ++t) a[t] = mfma16(w1f[t][ks], hf, a[t]);
        }
        const f32x2 mr = *(LAS f32x2*)(L_MR + (w * 64 + m * 16 + t4) * 8);
#pragma unroll
        for (int t = 0; t < 3; ++t) {
          const float bb[4] = {blo(b1r[t].x), bhi(b1r[t].x), blo(b1r[t].y), bhi(b1r[t].y)};
          float y[4];
#pragma unroll
          for (int i = 0; i < 4; ++i) {
            const float v = fmaf(a[t][i], mr.y, fmaf(mr.x, c1[t][i], bb[i]));
            y[i] = v * __builtin_amdgcn_rcpf(1.f + __builtin_amdgcn_exp2f(v * fmaf(v * v, K2, K1)));
          }
          hq[e][t][0] = pk(y[0], y[1]); hq[e][t][1] = pk(y[2], y[3]);
        }
      }
#pragma unroll
      for (int t = 0; t < 3; ++t) {
        swap16(hq[0][t][0], hq[1][t][0]); swap16(hq[0][t][1], hq[1][t][1]);
        st16(ws, u32x4{hq[0][t][0], hq[0][t][1], hq[1][t][0], hq[1][t][1]}, vL[t], WS_H1 + ((p * 2 + tt) * 88 + sW1[t]) * 1024);
      }
    }
  }
  TS(14);
  VMW(0);
  BAR();
  set_flag(ws, WS_FLAG + (4 * 256 + wg) * 128, tag, tid);
  TS(15);
  const rsrc_t rb2 = __builtin_amdgcn_make_buffer_rsrc((void*)b2, 0, 512 * 2, 0x00020000);
  const u32x4 b2r = __builtin_amdgcn_raw_buffer_load_b128(rb2, r8 * 16, g * 128, 0);
  FENCE();
  wait_flags(ws, vF, sF + 4 * 256 * 128, tag);
  TS(16);
  {
    u32x4 hf[2][22];
#pragma unroll
    for (int tt = 0; tt < 2; ++tt)
#pragma unroll
      for (int i = 0; i < 22; ++i) hf[tt][i] = ld16(ws, i >= 20 ? vT : l * 16, WS_H1 + ((p * 2 + tt) * 88 + w * 22 + i) * 1024);
    FENCE();
#pragma unroll
    for (int tt = 0; tt < 2; ++tt) {
      f32x16 a0 = {}, a1 = {};
#pragma unroll
      for (int i = 0; i < 22; ++i) { a0 = mfma32(w2f[2 * i], hf[tt][i], a0); a1 = mfma32(w2f[2 * i + 1], hf[tt][i], a1); }
#pragma unroll
      for (int q = 0; q < 4; ++q) {
        *(LAS f32x4*)(L_R + ((w * 4 + tt * 2) * 4 + q) * 1152 + l * 16) = f32x4{a0[4 * q], a0[4 * q + 1], a0[4 * q + 2], a0[4 * q + 3]};
        *(LAS f32x4*)(L_R + ((w * 4 + tt * 2 + 1) * 4 + q) * 1152 + l * 16) = f32x4{a1[4 * q], a1[4 * q + 1], a1[4 * q + 2], a1[4 * q + 3]};
      }
    }
    BAR();
    TS(17);
    const rsrc_t ro = __builtin_amdgcn_make_buffer_rsrc(out, 0, 2048 * 512 * 2, 0x00020000);
#pragma unroll
    for (int tt = 0; tt < 2; ++tt) {
      f32x4 lo = {blo(hb[tt][0]) + blo(b2r[0]), bhi(hb[tt][0]) + bhi(b2r[0]), blo(hb[tt][1]) + blo(b2r[1]), bhi(hb[tt][1]) + bhi(b2r[1])};
      f32x4 hi = {blo(hb[tt][2]) + blo(b2r[2]), bhi(hb[tt][2]) + bhi(b2r[2]), blo(hb[tt][3]) + blo(b2r[3]), bhi(hb[tt][3]) + bhi(b2r[3])};
#pragma unroll
      for (int s = 0; s < 4; ++s) {
        lo += *(LAS f32x4*)(aRr + (s * 16 + tt * 8) * 1152);
        hi += *(LAS f32x4*)(aRr + (s * 16 + tt * 8) * 1152 + 512);
      }
      __builtin_amdgcn_raw_buffer_store_b128(u32x4{pk(lo[0], lo[1]), pk(lo[2], lo[3]), pk(hi[0], hi[1]), pk(hi[2], hi[3])}, ro, s8 * 1024 + r8 * 16,
                                             (64 * p + 32 * tt + 8 * w) * 1024 + g * 128, 0);
    }
  }
  TS(18);
}

extern "C" void kernel_launch(void* const* d_in, const int* in_sizes, int n_in, void* d_out, int out_size, void* d_ws, size_t ws_size, hipStream_t stream) {
  (void)hipFuncSetAttribute((const void*)hh_block, hipFuncAttributeMaxDynamicSharedMemorySize, L_END);
  hh_block<<<256, 256, L_END, stream>>>((const u16*)d_in[0], (const u16*)d_in[1], (const u16*)d_in[2], (const u16*)d_in[3], (const u16*)d_in[5],
                                        (const u16*)d_in[6], (const u16*)d_in[7], (const u16*)d_in[8], (u16*)d_out, (char*)d_ws);
}
```

```cpp
#include <hip/hip_runtime.h>
#include <hip/hip_bf16.h>
#pragma clang diagnostic ignored "-Wint-to-pointer-cast"

typedef unsigned short u16;
typedef unsigned long long u64;
typedef __attribute__((ext_vector_type(8))) __bf16 bf16x8;
typedef __attribute__((ext_vector_type(2))) __bf16 bf16x2;
typedef __attribute__((ext_vector_type(2))) float f32x2;
typedef __attribute__((ext_vector_type(4))) float f32x4;
typedef __attribute__((ext_vector_type(16))) float f32x16;
typedef __attribute__((ext_vector_type(4))) unsigned u32x4;
typedef __attribute__((ext_vector_type(2))) unsigned u32x2;
typedef __attribute__((ext_vector_type(4))) short s16x4;
#define LAS __attribute__((address_space(3)))
#define DEV __device__ __forceinline__

constexpr unsigned WS_EPOCH = 0;
constexpr unsigned WS_FLAG  = 4096;
constexpr unsigned WS_WO    = WS_FLAG + 8 * 256 * 128;
constexpr unsigned WS_W2    = WS_WO + 8 * 32 * 2 * 1024;
constexpr unsigned WS_W1    = WS_W2 + 8 * 88 * 2 * 1024;
constexpr unsigned WS_KV    = WS_W1 + 86 * 16 * 1024;
constexpr unsigned WS_O     = WS_KV + 32 * 32 * 1152;
constexpr unsigned WS_H     = WS_O + 32 * 64 * 1024;
constexpr unsigned WS_HS    = WS_H + 32 * 64 * 1024;
constexpr unsigned WS_H1    = WS_HS + 32 * 4096;
constexpr unsigned WS_END   = WS_H1 + 32 * 2 * 88 * 1024;
constexpr unsigned WS_DBG   = 32u << 20;
constexpr unsigned OOB      = 0x7ff00000u;

constexpr unsigned L_X    = 0;
constexpr unsigned L_RING = 65536;
constexpr unsigned L_T0   = 131072;
constexpr unsigned L_T1   = L_T0 + 8192;
constexpr unsigned L_PHI  = L_T1 + 8192;
constexpr unsigned L_ST   = L_PHI + 4096;
constexpr unsigned L_MR   = L_ST + 4096;
constexpr unsigned L_EP   = L_T0;
constexpr unsigned L_H    = 0;
constexpr unsigned L_HS   = 65536;
constexpr unsigned L_R    = 73728;
constexpr unsigned L_END  = 160 * 1024;
static_assert(L_MR + 2048 <= L_END && L_R + 64 * 1152 <= L_PHI, "lds");

constexpr unsigned SPIN_CAP = 1u << 16;

DEV unsigned pk(float a, float b) { bf16x2 v = {(__bf16)a, (__bf16)b}; return __builtin_bit_cast(unsigned, v); }
DEV float blo(unsigned v) { return __builtin_bit_cast(float, v << 16); }
DEV float bhi(unsigned v) { return __builtin_bit_cast(float, v & 0xffff0000u); }
DEV f32x16 mfma32(u32x4 a, u32x4 b, f32x16 c) { return __builtin_amdgcn_mfma_f32_32x32x16_bf16(__builtin_bit_cast(bf16x8, a), __builtin_bit_cast(bf16x8, b), c, 0, 0, 0); }
DEV f32x4 mfma16(u32x4 a, u32x4 b, f32x4 c) { return __builtin_amdgcn_mfma_f32_16x16x32_bf16(__builtin_bit_cast(bf16x8, a), __builtin_bit_cast(bf16x8, b), c, 0, 0, 0); }
DEV u32x2 trrd(unsigned a) { return __builtin_bit_cast(u32x2, __builtin_amdgcn_ds_read_tr16_b64_v4i16((LAS s16x4*)a)); }
DEV u32x4 trfrag(unsigned a0, unsigned a1) { u32x2 x = trrd(a0), y = trrd(a1); return u32x4{x.x, x.y, y.x, y.y}; }
DEV u32x4 lrd(unsigned a) { return *(LAS u32x4*)a; }
DEV void lwr(unsigned a, u32x4 v) { *(LAS u32x4*)a = v; }
DEV float dot2(unsigned a, unsigned b, float c) { return __builtin_amdgcn_fdot2_f32_bf16(__builtin_bit_cast(bf16x2, a), __builtin_bit_cast(bf16x2, b), c, false); }
DEV u32x4 srd(const void* p, unsigned bytes) { u64 a = (u64)p; return u32x4{(unsigned)a, (unsigned)(a >> 32) & 0xffffu, bytes, 0x00020000u}; }
DEV void glds(u32x4 rs, unsigned lds, unsigned voff, unsigned soff) {
  asm volatile("s_mov_b32 m0, %0\n\ts_nop 0\n\tbuffer_load_dwordx4 %1, %2, %3 offen lds" ::"s"(lds), "v"(voff), "s"(rs), "s"(soff) : "memory");
}
DEV void glds_dev(u32x4 rs, unsigned lds, unsigned voff, unsigned soff) {
  asm volatile("s_mov_b32 m0, %0\n\ts_nop 0\n\tbuffer_load_dwordx4 %1, %2, %3 offen sc1 lds" ::"s"(lds), "v"(voff), "s"(rs), "s"(soff) : "memory");
}
#define VMW(n) asm volatile("s_waitcnt vmcnt(" #n ")" ::: "memory")
#define BAR() asm volatile("s_waitcnt lgkmcnt(0)\n\ts_barrier" ::: "memory")
#define FENCE() do { asm volatile("" ::: "memory"); __builtin_amdgcn_sched_barrier(0); } while (0)
typedef __amdgpu_buffer_rsrc_t rsrc_t;
DEV u32x4 ld16(rsrc_t r, unsigned v, unsigned s) { return __builtin_amdgcn_raw_buffer_load_b128(r, v, s, 16); }
DEV void st16(rsrc_t r, u32x4 d, unsigned v, unsigned s) { __builtin_amdgcn_raw_buffer_store_b128(d, r, v, s, 16); }
DEV void swap16(unsigned& a, unsigned& b) { auto r = __builtin_amdgcn_permlane16_swap(a, b, false, false); a = r[0]; b = r[1]; }
DEV float xsum16(float v) { unsigned a = __builtin_bit_cast(unsigned, v); auto r = __builtin_amdgcn_permlane16_swap(a, a, false, false); return __builtin_bit_cast(float, (unsigned)r[0]) + __builtin_bit_cast(float, (unsigned)r[1]); }
DEV float xsum32(float v) { unsigned a = __builtin_bit_cast(unsigned, v); auto r = __builtin_amdgcn_permlane32_swap(a, a, false, false); return __builtin_bit_cast(float, (unsigned)r[0]) + __builtin_bit_cast(float, (unsigned)r[1]); }
DEV float xsum8(float v) { return v + __builtin_bit_cast(float, __builtin_amdgcn_update_dpp(0, __builtin_bit_cast(int, v), 0x128, 0xf, 0xf, true)); }

DEV void wait_flags(rsrc_t ws, unsigned voff, unsigned soff, u32x2 tag) {
  unsigned sp = 0;
#pragma nounroll
  for (;;) {
    const u32x2 f = __builtin_amdgcn_raw_buffer_load_b64(ws, voff, soff, 16);
    if (__builtin_amdgcn_ballot_w64(((f.x ^ tag.x) | (f.y ^ tag.y)) != 0u) == 0ull || ++sp >= SPIN_CAP) break;
    __builtin_amdgcn_s_sleep(1);
  }
  FENCE();
}
DEV void set_flag(rsrc_t ws, unsigned off, u32x2 tag, int tid) {
  if (tid == 0) __builtin_amdgcn_raw_buffer_store_b64(tag, ws, off, 0, 16);
  FENCE();
}

DEV void softmax16(const f32x16& p, float c, unsigned (&o)[8]) {
  float m = fmaxf(fmaxf(p[0], p[1]), p[2]);
#pragma unroll
  for (int i = 3; i < 15; i += 2) m = fmaxf(fmaxf(m, p[i]), p[i + 1]);
  m = fmaxf(m, p[15]);
  float nb = -m * c, e[16], s = 0.f;
#pragma unroll
  for (int i = 0; i < 16; ++i) { e[i] = __builtin_amdgcn_exp2f(fmaf(p[i], c, nb)); s += e[i]; }
  float inv = __builtin_amdgcn_rcpf(s);
#pragma unroll
  for (int i = 0; i < 8; ++i) o[i] = pk(e[2 * i] * inv, e[2 * i + 1] * inv);
}

#define TS(i) do { if ((i) == 0) pt0 = __builtin_amdgcn_s_memrealtime(); if ((i) == 0) pt1 = __builtin_amdgcn_s_memrealtime(); } while (0)

__global__ __launch_bounds__(256, 1) void hh_block(const u16* __restrict__ x, const u16* __restrict__ wqkv, const u16* __restrict__ wphi,
                                                   const u16* __restrict__ wo, const u16* __restrict__ w1, const u16* __restrict__ b1,
                                                   const u16* __restrict__ w2, const u16* __restrict__ b2, u16* __restrict__ out, char* __restrict__ wsp) {
  const int tid = threadIdx.x, l = tid & 63;
  const unsigned w = __builtin_amdgcn_readfirstlane(tid >> 6);
  const unsigned b = blockIdx.x, p = (b & 7) * 4 + (b >> 6), g = (b >> 3) & 7, wg = p * 8 + g;
  u64 pt0 = 0, pt1 = 0;
  TS(0);
  const rsrc_t ws = __builtin_amdgcn_make_buffer_rsrc(wsp, 0, WS_END, 0x00020000);
  const u32x4 sx = srd(x, 2048 * 512 * 2), sw = srd(wqkv, 512 * 1536 * 2);
  const unsigned t0 = g < 6 ? 11 * g : 66 + 10 * (g - 6), nt = g < 6 ? 11 : 10;

  const unsigned r8 = l >> 3, s8 = l & 7;
  unsigned vX[2], vW[2];
#pragma unroll
  for (int e = 0; e < 2; ++e) {
    vX[e] = (64 * p + 16 * w + 8 * e + r8) * 1024 + ((s8 ^ (4 * e + (r8 >> 1))) << 4);
    vW[e] = r8 * 3072 + g * 128 + (((((s8 >> 1) ^ ((r8 & 2) | e)) << 1) | (s8 & 1)) << 4);
  }
  auto stage = [&](int S) {
    const unsigned slot = L_RING + (S & 3) * 16384;
    if (S < 8) {
      if (S < 4) {
#pragma unroll
        for (int e = 0; e < 2; ++e) glds(sx, L_X + (2 * S) * 8192 + (2 * w + e) * 1024, vX[e], (2 * S) * 128);
      }
#pragma unroll
      for (int e = 0; e < 2; ++e)
#pragma unroll
        for (int sg = 0; sg < 2; ++sg) glds(sw, slot + sg * 8192 + (2 * w + e) * 1024, vW[sg], (S * 64 + 16 * w + 8 * e) * 3072 + (1 + sg) * 1024);
      if (S < 4) {
#pragma unroll
        for (int e = 0; e < 2; ++e) glds(sx, L_X + (2 * S + 1) * 8192 + (2 * w + e) * 1024, vX[e], (2 * S + 1) * 128);
      }
    } else {
#pragma unroll
      for (int e = 0; e < 4; ++e) glds(sw, slot + (4 * w + e) * 1024, vW[e & 1], ((S - 8) * 128 + 32 * w + 8 * e) * 3072);
    }
  };
  stage(0);
  const unsigned tks = w == 0 ? p : p + 32 * (w - 1);
  {
    const u32x4 st = w == 0 ? srd(wo, 512 * 512 * 2) : srd(w2, 1376 * 512 * 2);
#pragma unroll
    for (int e = 0; e < 2; ++e) glds(st, L_T0 + w * 2048 + e * 1024, r8 * 1024 + g * 128 + s8 * 16, ((tks < 86 ? tks : 85) * 16 + 8 * e) * 1024);
    const u32x4 s1 = srd(w1, 512 * 1376 * 2);
    const unsigned v1 = (l & 31) < 2 * nt ? (l >> 5) * 2752 + t0 * 32 + (l & 31) * 16 : OOB;
#pragma unroll
    for (int e = 0; e < 2; ++e) glds(s1, L_T1 + (2 * w + e) * 1024, v1, (16 * p + 4 * w + 2 * e) * 2752);
    glds(srd(wphi, 32 * 256 * 2), L_PHI + w * 1024, l < 32 ? l * 16 : OOB, (4 * g + w) * 512);
  }
  stage(1);
  stage(2);
  const u64 ep = *(const __attribute__((address_space(4))) u64*)(wsp + WS_EPOCH) + 1;
  const u32x2 tag = {(unsigned)ep, (unsigned)(ep >> 32)};

  const unsigned a4 = l >> 4, q4 = (l >> 2) & 3, p4 = l & 3, h = l >> 5, t5 = l & 31, t4 = l & 15;
  const unsigned aW = (a4 & 1) * 8192 + h * 1024 + q4 * 128 + ((w ^ ((q4 & 2) | (a4 & 1))) << 5) + ((((p4 & 1) << 1) | (p4 >> 1)) << 3);
  const unsigned aX = t5 * 128 + ((h ^ ((t5 >> 1) & 7)) << 4);
  const u32x4 ones = {0x3f803f80u, 0x3f803f80u, 0x3f803f80u, 0x3f803f80u};
  f32x16 acc[2] = {}, cs = {};
  float su[2] = {0.f, 0.f}, sq[2] = {0.f, 0.f};
  float mean5[2], rstd5[2], mean4[4];
#pragma unroll
  for (int S = 0; S < 8; ++S) {
    if (S == 0) VMW(21); else if (S == 1) VMW(16); else if (S == 2) VMW(12); else VMW(8);
    BAR();
    TS(20 + S);
    stage(S + 3);
    const unsigned slot = L_RING + (S & 3) * 16384;
#pragma unroll
    for (int ks = 0; ks < 4; ++ks) {
      u32x4 wf = trfrag(slot + aW + ks * 2048, slot + aW + ks * 2048 + 512);
#pragma unroll
      for (int tt = 0; tt < 2; ++tt) acc[tt] = mfma32(wf, lrd(L_X + S * 8192 + tt * 4096 + (aX ^ (ks * 32))), acc[tt]);
      cs = mfma32(wf, ones, cs);
    }
    if (S < 4) {
#pragma unroll
      for (int c = 0; c < 2; ++c)
#pragma unroll
        for (int tt = 0; tt < 2; ++tt) {
          u32x4 v = lrd(L_X + (2 * S + c) * 8192 + tt * 4096 + (aX ^ (w * 32)));
#pragma unroll
          for (int i = 0; i < 4; ++i) { su[tt] = dot2(v[i], 0x3f803f80u, su[tt]); sq[tt] = dot2(v[i], v[i], sq[tt]); }
        }
    }
    if (S == 1) {
#pragma unroll
      for (int ct = 0; ct < 2; ++ct) {
        const unsigned a = L_T0 + w * 2048 + (8 * h + q4) * 128 + ct * 64 + (a4 & 1) * 32 + p4 * 8;
        const unsigned dst = w == 0 ? WS_WO + ((g * 32 + tks) * 2 + ct) * 1024 : WS_W2 + ((g * 88 + tks) * 2 + ct) * 1024;
        st16(ws, trfrag(a, a + 512), tks < 86 ? l * 16 : OOB, dst);
      }
#pragma unroll
      for (int e = 0; e < 2; ++e) {
        const unsigned tl = 2 * (w + 4 * e) + h;
        const unsigned a = L_T1 + (8 * (a4 & 1) + q4) * 512 + tl * 32 + p4 * 8;
        st16(ws, trfrag(a, a + 2048), tl < nt ? (t0 + tl) * 16384 + t5 * 16 : OOB, WS_W1 + (p >> 1) * 1024 + (p & 1) * 512);
      }
    }
    if (S == 3) {
#pragma unroll
      for (int tt = 0; tt < 2; ++tt) *(LAS f32x2*)(L_ST + ((w * 2 + h) * 64 + tt * 32 + t5) * 8) = f32x2{su[tt], sq[tt]};
    }
    if (S == 4) {
      f32x2 a = {0.f, 0.f};
#pragma unroll
      for (int i = 0; i < 8; ++i) a += *(LAS f32x2*)(L_ST + (i * 64 + l) * 8);
      const float mean = a.x * (1.f / 512), var = fmaxf(a.y * (1.f / 512) - mean * mean, 0.f);
      *(LAS f32x2*)(L_MR + (w * 64 + l) * 8) = f32x2{mean, __builtin_amdgcn_rsqf(var + 1e-5f)};
    }
    if (S == 5) {
#pragma unroll
      for (int tt = 0; tt < 2; ++tt) { f32x2 a = *(LAS f32x2*)(L_MR + (w * 64 + tt * 32 + t5) * 8); mean5[tt] = a.x; rstd5[tt] = a.y; }
#pragma unroll
      for (int m = 0; m < 4; ++m) mean4[m] = *(LAS float*)(L_MR + (w * 64 + m * 16 + t4) * 8);
    }
  }
  TS(1);
  u32x4 pf;
  {
    const unsigned a = L_PHI + w * 1024 + (8 * h + q4) * 32 + (2 * (a4 & 1) + (p4 >> 1)) * 8;
    pf = trfrag(a, a + 128) ^ ((l & 4) ? 0x80008000u : 0u);
  }
  constexpr float LOG2E = 1.4426950408889634f;
  {
    f32x4 kv0 = {}, kv1 = {}, z0 = {}, z1 = {};
    const unsigned ePH = L_EP + w * 4096, eV = ePH + 2560;
#pragma unroll
    for (int tt = 0; tt < 2; ++tt) {
      float kk[16];
#pragma unroll
      for (int i = 0; i < 16; ++i) kk[i] = fmaf(-mean5[tt], cs[i], acc[tt][i]);
      f32x16 pp = mfma32(pf, u32x4{pk(kk[0], kk[1]), pk(kk[2], kk[3]), pk(kk[4], kk[5]), pk(kk[6], kk[7])}, f32x16{});
      const float rs = rstd5[tt];
      lwr(eV + t5 * 48 + h * 16, u32x4{pk(kk[8] * rs, kk[9] * rs), pk(kk[10] * rs, kk[11] * rs), pk(kk[12] * rs, kk[13] * rs), pk(kk[14] * rs, kk[15] * rs)});
      unsigned ph[8];
      softmax16(pp, rs * LOG2E, ph);
      lwr(ePH + t5 * 80 + h * 32, u32x4{ph[0], ph[1], ph[2], ph[3]});
      lwr(ePH + t5 * 80 + h * 32 + 16, u32x4{ph[4], ph[5], ph[6], ph[7]});
      const unsigned aP = ePH + (8 * a4 + q4) * 80 + p4 * 16, aV = eV + (8 * a4 + q4) * 48 + p4 * 8;
      u32x4 f0 = trfrag(aP, aP + 320), f1 = trfrag(aP + 8, aP + 328), fv = trfrag(aV, aV + 192);
      kv0 = mfma16(f0, fv, kv0); kv1 = mfma16(f1, fv, kv1);
      z0 = mfma16(f0, ones, z0); z1 = mfma16(f1, ones, z1);
    }
    VMW(0);
    BAR();
    stage(11);
    const unsigned dst = WS_KV + ((4 * g + w) * 32 + p) * 1152;
    st16(ws, u32x4{pk(kv0[0], kv0[1]), pk(kv0[2], kv0[3]), pk(kv1[0], kv1[1]), pk(kv1[2], kv1[3])}, l * 16, dst);
    st16(ws, u32x4{pk(z0[0], z0[1]), pk(z0[2], z0[3]), pk(z1[0], z1[1]), pk(z1[2], z1[3])}, t4 == 0 ? 1024 + a4 * 16 : OOB, dst);
    FENCE();
  }
  TS(2);
  u32x4 pq[4];
  u32x4 xr[2];
  {
    f32x4 aq[4] = {}, cq = {};
    const unsigned aWq = a4 * 1024 + q4 * 128 + ((w ^ ((q4 & 2) | (a4 & 1))) << 5) + p4 * 8;
    const unsigned aXq = t4 * 128 + ((a4 ^ (t4 >> 1)) << 4);
#pragma unroll
    for (int S = 8; S < 12; ++S) {
      if (S == 10) { VMW(0); BAR(); set_flag(ws, WS_FLAG + (1 * 256 + wg) * 128, tag, tid); TS(3); }
      const unsigned slot = L_RING + (S & 3) * 16384;
#pragma unroll
      for (int ks = 0; ks < 4; ++ks) {
        const int K2 = 4 * (S - 8) + ks;
        u32x4 wf = trfrag(slot + aWq + ks * 4096, slot + aWq + ks * 4096 + 512);
#pragma unroll
        for (int m = 0; m < 4; ++m) aq[m] = mfma16(wf, lrd(L_X + (K2 >> 1) * 8192 + m * 2048 + (aXq ^ ((K2 & 1) * 64))), aq[m]);
        cq = mfma16(wf, ones, cq);
      }
    }
#pragma unroll
    for (int tt = 0; tt < 2; ++tt) {
      const unsigned row = 32 * tt + 8 * w + s8;
      xr[tt] = lrd(L_X + g * 8192 + row * 128 + ((r8 ^ ((row >> 1) & 7)) << 4));
      unsigned qa[2][2];
#pragma unroll
      for (int e = 0; e < 2; ++e) {
        const f32x4 a = aq[2 * tt + e]; const float mu = mean4[2 * tt + e];
        qa[e][0] = pk(fmaf(-mu, cq[0], a[0]), fmaf(-mu, cq[1], a[1]));
        qa[e][1] = pk(fmaf(-mu, cq[2], a[2]), fmaf(-mu, cq[3], a[3]));
      }
      swap16(qa[0][0], qa[1][0]); swap16(qa[0][1], qa[1][1]);
      f32x16 pp = mfma32(pf, u32x4{qa[0][0], qa[0][1], qa[1][0], qa[1][1]}, f32x16{});
      unsigned ph[8];
      softmax16(pp, rstd5[tt] * LOG2E, ph);
#pragma unroll
      for (int i = 0; i < 4; ++i) swap16(ph[i], ph[4 + i]);
      pq[2 * tt] = u32x4{ph[0], ph[1], ph[2], ph[3]};
      pq[2 * tt + 1] = u32x4{ph[4], ph[5], ph[6], ph[7]};
    }
  }
  TS(4);
  if (w == 0) wait_flags(ws, t5 * 1024, WS_FLAG + (1 * 256 + g) * 128, tag);
  BAR();
  TS(5);
  {
    const unsigned src = WS_KV + (4 * g + w) * 32 * 1152;
    u32x4 zf[2], kp[32];
#pragma unroll
    for (int i = 0; i < 2; ++i) zf[i] = ld16(ws, t4 * 1152 + 1024 + a4 * 16, src + i * 16 * 1152);
#pragma unroll
    for (int i = 0; i < 32; ++i) kp[i] = ld16(ws, l * 16, src + i * 1152);
    FENCE();
    float inv[4];
#pragma unroll
    for (int m = 0; m < 4; ++m) {
      f32x4 d = mfma16(zf[1], pq[m], mfma16(zf[0], pq[m], f32x4{}));
      inv[m] = __builtin_amdgcn_rcpf(xsum32(xsum16((d[0] + d[1]) + (d[2] + d[3]))) + 1e-6f);
    }
    const unsigned one = (a4 == q4) ? ((p4 & 1) ? 0x3f800000u : 0x00003f80u) : 0u;
    const unsigned e0 = (p4 & 2) ? 0u : one, e1 = (p4 & 2) ? one : 0u;
    const u32x4 sel0 = {e0, e1, 0u, 0u}, sel1 = {0u, 0u, e0, e1};
    f32x4 k0 = {}, k1 = {};
#pragma unroll
    for (int i = 0; i < 32; ++i) { k0 = mfma16(sel0, kp[i], k0); k1 = mfma16(sel1, kp[i], k1); }
    const u32x4 kf = {pk(k0[0], k0[1]), pk(k0[2], k0[3]), pk(k1[0], k1[1]), pk(k1[2], k1[3])};
#pragma unroll
    for (int tt = 0; tt < 2; ++tt) {
      unsigned oa[2][2];
#pragma unroll
      for (int e = 0; e < 2; ++e) {
        const f32x4 n = mfma16(kf, pq[2 * tt + e], f32x4{}); const float iv = inv[2 * tt + e];
        oa[e][0] = pk(n[0] * iv, n[1] * iv); oa[e][1] = pk(n[2] * iv, n[3] * iv);
      }
      swap16(oa[0][0], oa[1][0]); swap16(oa[0][1], oa[1][1]);
      st16(ws, u32x4{oa[0][0], oa[0][1], oa[1][0], oa[1][1]}, l * 16, WS_O + ((p * 32 + 4 * g + w) * 2 + tt) * 1024);
    }
  }
  TS(6);
  VMW(0);
  BAR();
  set_flag(ws, WS_FLAG + (2 * 256 + wg) * 128, tag, tid);
  TS(7);
  u32x4 wof[16], w1f[3][16], w2f[44];
  unsigned sW1[3], vL[3];
  {
    const unsigned tb = t0 + ((nt == 10 && w == 3) ? 8 : 3 * w);
    const bool ok = w < (nt == 10 ? 2 : 3);
    sW1[0] = tb; sW1[1] = tb + 1; sW1[2] = ok ? tb + 2 : tb;
    vL[0] = vL[1] = l * 16; vL[2] = ok ? l * 16 : OOB;
  }
  const unsigned vT = w == 3 ? OOB : l * 16;
#pragma unroll
  for (int i = 0; i < 16; ++i) wof[i] = ld16(ws, l * 16, WS_WO + (g * 64 + w * 16 + i) * 1024);
#pragma unroll
  for (int i = 0; i < 16; ++i) w1f[0][i] = ld16(ws, l * 16, WS_W1 + sW1[0] * 16384 + i * 1024);
  FENCE();
  const unsigned vF = (l & 1) * 128, sF = WS_FLAG + (p * 8 + 2 * w) * 128;
  wait_flags(ws, vF, sF + 2 * 256 * 128, tag);
  TS(8);
  f32x16 ac[2][2] = {};
  unsigned hb[2][4];
  const unsigned aRr = L_R + r8 * 1152 + (8 * w + s8) * 16;
  {
    u32x4 of[16];
#pragma unroll
    for (int i = 0; i < 16; ++i) of[i] = ld16(ws, l * 16, WS_O + (p * 64 + w * 16 + i) * 1024);
    FENCE();
#pragma unroll
    for (int ks = 0; ks < 8; ++ks)
#pragma unroll
      for (int tt = 0; tt < 2; ++tt)
#pragma unroll
        for (int ct = 0; ct < 2; ++ct) ac[tt][ct] = mfma32(wof[2 * ks + ct], of[2 * ks + tt], ac[tt][ct]);
#pragma unroll
    for (int u = 0; u < 4; ++u)
#pragma unroll
      for (int q = 0; q < 4; ++q) {
        const f32x16& a = ac[u >> 1][u & 1];
        *(LAS f32x4*)(L_R + ((w * 4 + u) * 4 + q) * 1152 + l * 16) = f32x4{a[4 * q], a[4 * q + 1], a[4 * q + 2], a[4 * q + 3]};
      }
    BAR();
    TS(9);
    if (b == 0 && tid == 0) __builtin_amdgcn_raw_buffer_store_b64(tag, ws, WS_EPOCH, 0, 16);
    f32x2 stt;
#pragma unroll
    for (int tt = 0; tt < 2; ++tt) {
      f32x4 lo = {blo(xr[tt][0]), bhi(xr[tt][0]), blo(xr[tt][1]), bhi(xr[tt][1])}, hi = {blo(xr[tt][2]), bhi(xr[tt][2]), blo(xr[tt][3]), bhi(xr[tt][3])};
#pragma unroll
      for (int s = 0; s < 4; ++s) {
        lo += *(LAS f32x4*)(aRr + (s * 16 + tt * 8) * 1152);
        hi += *(LAS f32x4*)(aRr + (s * 16 + tt * 8) * 1152 + 512);
      }
      hb[tt][0] = pk(lo[0], lo[1]); hb[tt][1] = pk(lo[2], lo[3]); hb[tt][2] = pk(hi[0], hi[1]); hb[tt][3] = pk(hi[2], hi[3]);
      st16(ws, u32x4{hb[tt][0], hb[tt][1], hb[tt][2], hb[tt][3]}, (r8 >> 2) * 1024 + ((r8 & 3) * 16 + s8) * 16,
           WS_H + ((p * 4 + 2 * tt + (w >> 1)) * 16 + 2 * g) * 1024 + (w & 1) * 128);
      float a = 0.f, c = 0.f;
#pragma unroll
      for (int i = 0; i < 4; ++i) { a = dot2(hb[tt][i], 0x3f803f80u, a); c = dot2(hb[tt][i], hb[tt][i], c); }
      a = xsum32(xsum16(xsum8(a))); c = xsum32(xsum16(xsum8(c)));
      if (tt == 0 || r8 == 1) stt = f32x2{a, c};
    }
    __builtin_amdgcn_raw_buffer_store_b64(__builtin_bit_cast(u32x2, stt), ws, l < 16 ? l * 8 : OOB, WS_HS + wg * 512 + w * 128, 16);
  }
  TS(10);
  VMW(0);
  BAR();
  set_flag(ws, WS_FLAG + (3 * 256 + wg) * 128, tag, tid);
  TS(11);
  u32x2 b1r[3];
  {
    const rsrc_t rb1 = __builtin_amdgcn_make_buffer_rsrc((void*)b1, 0, 1376 * 2, 0x00020000);
#pragma unroll
    for (int t = 0; t < 3; ++t) b1r[t] = __builtin_amdgcn_raw_buffer_load_b64(rb1, a4 * 8, sW1[t] * 32, 0);
  }
#pragma unroll
  for (int t = 1; t < 3; ++t)
#pragma unroll
    for (int i = 0; i < 16; ++i) w1f[t][i] = ld16(ws, vL[t], WS_W1 + sW1[t] * 16384 + i * 1024);
  FENCE();
  wait_flags(ws, vF, sF + 3 * 256 * 128, tag);
  TS(12);
  {
    const u32x4 sws = srd(wsp, WS_END);
    glds_dev(sws, L_HS + w * 1024, l * 16, WS_HS + p * 4096 + w * 1024);
#pragma unroll
    for (int m = 0; m < 4; ++m)
#pragma unroll
      for (int e = 0; e < 4; ++e) glds_dev(sws, L_H + (m * 16 + 4 * w + e) * 1024, l * 16, WS_H + (p * 64 + m * 16 + 4 * w + e) * 1024);
    f32x4 c1[3] = {};
#pragma unroll
    for (int ks = 0; ks < 16; ++ks)
#pragma unroll
      for (int t = 0; t < 3; ++t) c1[t] = mfma16(w1f[t][ks], ones, c1[t]);
    asm volatile("" : "+v"(c1[0]), "+v"(c1[1]), "+v"(c1[2]));
    VMW(0);
    BAR();
    TS(13);
#pragma unroll
    for (int i = 0; i < 44; ++i) w2f[i] = ld16(ws, i >= 40 ? vT : l * 16, WS_W2 + (g * 176 + w * 44 + i) * 1024);
    FENCE();
    {
      f32x2 a = {0.f, 0.f};
      const unsigned sl = ((l >> 3) & 3) * 128 + (l >> 5) * 64 + (l & 7) * 8;
#pragma unroll
      for (int i = 0; i < 8; ++i) a += *(LAS f32x2*)(L_HS + i * 512 + sl);
      const float mean = a.x * (1.f / 512), var = fmaxf(a.y * (1.f / 512) - mean * mean, 0.f);
      const float rs = __builtin_amdgcn_rsqf(var + 1e-5f);
      *(LAS f32x2*)(L_MR + (w * 64 + l) * 8) = f32x2{-mean * rs, rs};
    }
    constexpr float K1 = -2.f * 0.7978845608028654f * LOG2E, K2 = K1 * 0.044715f;
#pragma unroll
    for (int tt = 0; tt < 2; ++tt) {
      unsigned hq[2][3][2];
#pragma unroll
      for (int e = 0; e < 2; ++e) {
        const int m = 2 * tt + e;
        f32x4 a[3] = {};
#pragma unroll
        for (int ks = 0; ks < 16; ++ks) {
          const u32x4 hf = lrd(L_H + (m * 16 + ks) * 1024 + l * 16);
#pragma unroll
          for (int t = 0; t < 3; ++t) a[t] = mfma16(w1f[t][ks], hf, a[t]);
        }
        const f32x2 mr = *(LAS f32x2*)(L_MR + (w * 64 + m * 16 + t4) * 8);
#pragma unroll
        for (int t = 0; t < 3; ++t) {
          const float bb[4] = {blo(b1r[t].x), bhi(b1r[t].x), blo(b1r[t].y), bhi(b1r[t].y)};
          float y[4];
#pragma unroll
          for (int i = 0; i < 4; ++i) {
            const float v = fmaf(a[t][i], mr.y, fmaf(mr.x, c1[t][i], bb[i]));
            y[i] = v * __builtin_amdgcn_rcpf(1.f + __builtin_amdgcn_exp2f(v * fmaf(v * v, K2, K1)));
          }
          hq[e][t][0] = pk(y[0], y[1]); hq[e][t][1] = pk(y[2], y[3]);
        }
      }
#pragma unroll
      for (int t = 0; t < 3; ++t) {
        swap16(hq[0][t][0], hq[1][t][0]); swap16(hq[0][t][1], hq[1][t][1]);
        st16(ws, u32x4{hq[0][t][0], hq[0][t][1], hq[1][t][0], hq[1][t][1]}, vL[t], WS_H1 + ((p * 2 + tt) * 88 + sW1[t]) * 1024);
      }
    }
  }
  TS(14);
  VMW(0);
  BAR();
  set_flag(ws, WS_FLAG + (4 * 256 + wg) * 128, tag, tid);
  TS(15);
  const rsrc_t rb2 = __builtin_amdgcn_make_buffer_rsrc((void*)b2, 0, 512 * 2, 0x00020000);
  const u32x4 b2r = __builtin_amdgcn_raw_buffer_load_b128(rb2, r8 * 16, g * 128, 0);
  FENCE();
  wait_flags(ws, vF, sF + 4 * 256 * 128, tag);
  TS(16);
  {
    u32x4 hf[2][22];
#pragma unroll
    for (int tt = 0; tt < 2; ++tt)
#pragma unroll
      for (int i = 0; i < 22; ++i) hf[tt][i] = ld16(ws, i >= 20 ? vT : l * 16, WS_H1 + ((p * 2 + tt) * 88 + w * 22 + i) * 1024);
    FENCE();
#pragma unroll
    for (int tt = 0; tt < 2; ++tt) {
      f32x16 a0 = {}, a1 = {};
#pragma unroll
      for (int i = 0; i < 22; ++i) { a0 = mfma32(w2f[2 * i], hf[tt][i], a0); a1 = mfma32(w2f[2 * i + 1], hf[tt][i], a1); }
#pragma unroll
      for (int q = 0; q < 4; ++q) {
        *(LAS f32x4*)(L_R + ((w * 4 + tt * 2) * 4 + q) * 1152 + l * 16) = f32x4{a0[4 * q], a0[4 * q + 1], a0[4 * q + 2], a0[4 * q + 3]};
        *(LAS f32x4*)(L_R + ((w * 4 + tt * 2 + 1) * 4 + q) * 1152 + l * 16) = f32x4{a1[4 * q], a1[4 * q + 1], a1[4 * q + 2], a1[4 * q + 3]};
      }
    }
    BAR();
    TS(17);
    const rsrc_t ro = __builtin_amdgcn_make_buffer_rsrc(out, 0, 2048 * 512 * 2, 0x00020000);
#pragma unroll
    for (int tt = 0; tt < 2; ++tt) {
      f32x4 lo = {blo(hb[tt][0]) + blo(b2r[0]), bhi(hb[tt][0]) + bhi(b2r[0]), blo(hb[tt][1]) + blo(b2r[1]), bhi(hb[tt][1]) + bhi(b2r[1])};
      f32x4 hi = {blo(hb[tt][2]) + blo(b2r[2]), bhi(hb[tt][2]) + bhi(b2r[2]), blo(hb[tt][3]) + blo(b2r[3]), bhi(hb[tt][3]) + bhi(b2r[3])};
#pragma unroll
      for (int s = 0; s < 4; ++s) {
        lo += *(LAS f32x4*)(aRr + (s * 16 + tt * 8) * 1152);
        hi += *(LAS f32x4*)(aRr + (s * 16 + tt * 8) * 1152 + 512);
      }
      __builtin_amdgcn_raw_buffer_store_b128(u32x4{pk(lo[0], lo[1]), pk(lo[2], lo[3]), pk(hi[0], hi[1]), pk(hi[2], hi[3])}, ro, s8 * 1024 + r8 * 16,
                                             (64 * p + 32 * tt + 8 * w) * 1024 + g * 128, 0);
    }
  }
  TS(18);
  { while(__builtin_amdgcn_s_memrealtime()-pt1<6000ull) {} }
}

extern "C" void kernel_launch(void* const* d_in, const int* in_sizes, int n_in, void* d_out, int out_size, void* d_ws, size_t ws_size, hipStream_t stream) {
  (void)hipFuncSetAttribute((const void*)hh_block, hipFuncAttributeMaxDynamicSharedMemorySize, L_END);
  hh_block<<<256, 256, L_END, stream>>>((const u16*)d_in[0], (const u16*)d_in[1], (const u16*)d_in[2], (const u16*)d_in[3], (const u16*)d_in[5],
                                        (const u16*)d_in[6], (const u16*)d_in[7], (const u16*)d_in[8], (u16*)d_out, (char*)d_ws);
}
```

```cpp
#include <hip/hip_runtime.h>
#include <hip/hip_bf16.h>
#pragma clang diagnostic ignored "-Wint-to-pointer-cast"

typedef unsigned short u16;
typedef unsigned long long u64;
typedef __attribute__((ext_vector_type(8))) __bf16 bf16x8;
typedef __attribute__((ext_vector_type(2))) __bf16 bf16x2;
typedef __attribute__((ext_vector_type(2))) float f32x2;
typedef __attribute__((ext_vector_type(4))) float f32x4;
typedef __attribute__((ext_vector_type(16))) float f32x16;
typedef __attribute__((ext_vector_type(4))) unsigned u32x4;
typedef __attribute__((ext_vector_type(2))) unsigned u32x2;
typedef __attribute__((ext_vector_type(4))) short s16x4;
#define LAS __attribute__((address_space(3)))
#define DEV __device__ __forceinline__

constexpr unsigned WS_EPOCH = 0;
constexpr unsigned WS_FLAG  = 4096;
constexpr unsigned WS_WO    = WS_FLAG + 8 * 256 * 128;
constexpr unsigned WS_W2    = WS_WO + 8 * 32 * 2 * 1024;
constexpr unsigned WS_W1    = WS_W2 + 8 * 88 * 2 * 1024;
constexpr unsigned WS_KV    = WS_W1 + 86 * 16 * 1024;
constexpr unsigned WS_O     = WS_KV + 32 * 32 * 1152;
constexpr unsigned WS_H     = WS_O + 32 * 64 * 1024;
constexpr unsigned WS_HS    = WS_H + 32 * 64 * 1024;
constexpr unsigned WS_H1    = WS_HS + 32 * 4096;
constexpr unsigned WS_END   = WS_H1 + 32 * 2 * 88 * 1024;
constexpr unsigned WS_DBG   = 32u << 20;
constexpr unsigned OOB      = 0x7ff00000u;

constexpr unsigned L_X    = 0;
constexpr unsigned L_RING = 65536;
constexpr unsigned L_T0   = 131072;
constexpr unsigned L_T1   = L_T0 + 8192;
constexpr unsigned L_PHI  = L_T1 + 8192;
constexpr unsigned L_ST   = L_PHI + 4096;
constexpr unsigned L_MR   = L_ST + 4096;
constexpr unsigned L_EP   = L_T0;
constexpr unsigned L_H    = 0;
constexpr unsigned L_HS   = 65536;
constexpr unsigned L_R    = 73728;
constexpr unsigned L_END  = 160 * 1024;
static_assert(L_MR + 2048 <= L_END && L_R + 64 * 1152 <= L_PHI, "lds");

constexpr unsigned SPIN_CAP = 1u << 16;

DEV unsigned pk(float a, float b) { bf16x2 v = {(__bf16)a, (__bf16)b}; return __builtin_bit_cast(unsigned, v); }
DEV float blo(unsigned v) { return __builtin_bit_cast(float, v << 16); }
DEV float bhi(unsigned v) { return __builtin_bit_cast(float, v & 0xffff0000u); }
DEV f32x16 mfma32(u32x4 a, u32x4 b, f32x16 c) { return __builtin_amdgcn_mfma_f32_32x32x16_bf16(__builtin_bit_cast(bf16x8, a), __builtin_bit_cast(bf16x8, b), c, 0, 0, 0); }
DEV f32x4 mfma16(u32x4 a, u32x4 b, f32x4 c) { return __builtin_amdgcn_mfma_f32_16x16x32_bf16(__builtin_bit_cast(bf16x8, a), __builtin_bit_cast(bf16x8, b), c, 0, 0, 0); }
DEV u32x2 trrd(unsigned a) { return __builtin_bit_cast(u32x2, __builtin_amdgcn_ds_read_tr16_b64_v4i16((LAS s16x4*)a)); }
DEV u32x4 trfrag(unsigned a0, unsigned a1) { u32x2 x = trrd(a0), y = trrd(a1); return u32x4{x.x, x.y, y.x, y.y}; }
DEV u32x4 lrd(unsigned a) { return *(LAS u32x4*)a; }
DEV void lwr(unsigned a, u32x4 v) { *(LAS u32x4*)a = v; }
DEV float dot2(unsigned a, unsigned b, float c) { return __builtin_amdgcn_fdot2_f32_bf16(__builtin_bit_cast(bf16x2, a), __builtin_bit_cast(bf16x2, b), c, false); }
DEV u32x4 srd(const void* p, unsigned bytes) { u64 a = (u64)p; return u32x4{(unsigned)a, (unsigned)(a >> 32) & 0xffffu, bytes, 0x00020000u}; }
DEV void glds(u32x4 rs, unsigned lds, unsigned voff, unsigned soff) {
  asm volatile("s_mov_b32 m0, %0\n\ts_nop 0\n\tbuffer_load_dwordx4 %1, %2, %3 offen lds" ::"s"(lds), "v"(voff), "s"(rs), "s"(soff) : "memory");
}
DEV void glds_dev(u32x4 rs, unsigned lds, unsigned voff, unsigned soff) {
  asm volatile("s_mov_b32 m0, %0\n\ts_nop 0\n\tbuffer_load_dwordx4 %1, %2, %3 offen sc1 lds" ::"s"(lds), "v"(voff), "s"(rs), "s"(soff) : "memory");
}
#define VMW(n) asm volatile("s_waitcnt vmcnt(" #n ")" ::: "memory")
#define BAR() asm volatile("s_waitcnt lgkmcnt(0)\n\ts_barrier" ::: "memory")
#define FENCE() do { asm volatile("" ::: "memory"); __builtin_amdgcn_sched_barrier(0); } while (0)
typedef __amdgpu_buffer_rsrc_t rsrc_t;
DEV u32x4 ld16(rsrc_t r, unsigned v, unsigned s) { return __builtin_amdgcn_raw_buffer_load_b128(r, v, s, 16); }
DEV void st16(rsrc_t r, u32x4 d, unsigned v, unsigned s) { __builtin_amdgcn_raw_buffer_store_b128(d, r, v, s, 16); }
DEV void swap16(unsigned& a, unsigned& b) { auto r = __builtin_amdgcn_permlane16_swap(a, b, false, false); a = r[0]; b = r[1]; }
DEV float xsum16(float v) { unsigned a = __builtin_bit_cast(unsigned, v); auto r = __builtin_amdgcn_permlane16_swap(a, a, false, false); return __builtin_bit_cast(float, (unsigned)r[0]) + __builtin_bit_cast(float, (unsigned)r[1]); }
DEV float xsum32(float v) { unsigned a = __builtin_bit_cast(unsigned, v); auto r = __builtin_amdgcn_permlane32_swap(a, a, false, false); return __builtin_bit_cast(float, (unsigned)r[0]) + __builtin_bit_cast(float, (unsigned)r[1]); }
DEV float xsum8(float v) { return v + __builtin_bit_cast(float, __builtin_amdgcn_update_dpp(0, __builtin_bit_cast(int, v), 0x128, 0xf, 0xf, true)); }

DEV void wait_flags(rsrc_t ws, unsigned voff, unsigned soff, u32x2 tag) {
  unsigned sp = 0;
#pragma nounroll
  for (;;) {
    const u32x2 f = __builtin_amdgcn_raw_buffer_load_b64(ws, voff, soff, 16);
    if (__builtin_amdgcn_ballot_w64(((f.x ^ tag.x) | (f.y ^ tag.y)) != 0u) == 0ull || ++sp >= SPIN_CAP) break;
    __builtin_amdgcn_s_sleep(1);
  }
  FENCE();
}
DEV void set_flag(rsrc_t ws, unsigned off, u32x2 tag, int tid) {
  if (tid == 0) __builtin_amdgcn_raw_buffer_store_b64(tag, ws, off, 0, 16);
  FENCE();
}

DEV void softmax16(const f32x16& p, float c, unsigned (&o)[8]) {
  float m = fmaxf(fmaxf(p[0], p[1]), p[2]);
#pragma unroll
  for (int i = 3; i < 15; i += 2) m = fmaxf(fmaxf(m, p[i]), p[i + 1]);
  m = fmaxf(m, p[15]);
  float nb = -m * c, e[16], s = 0.f;
#pragma unroll
  for (int i = 0; i < 16; ++i) { e[i] = __builtin_amdgcn_exp2f(fmaf(p[i], c, nb)); s += e[i]; }
  float inv = __builtin_amdgcn_rcpf(s);
#pragma unroll
  for (int i = 0; i < 8; ++i) o[i] = pk(e[2 * i] * inv, e[2 * i + 1] * inv);
}

#define TS(i)

__global__ __launch_bounds__(256, 1) void hh_block(const u16* __restrict__ x, const u16* __restrict__ wqkv, const u16* __restrict__ wphi,
                                                   const u16* __restrict__ wo, const u16* __restrict__ w1, const u16* __restrict__ b1,
                                                   const u16* __restrict__ w2, const u16* __restrict__ b2, u16* __restrict__ out, char* __restrict__ wsp) {
  const int tid = threadIdx.x, l = tid & 63;
  const unsigned w = __builtin_amdgcn_readfirstlane(tid >> 6);
  const unsigned b = blockIdx.x, p = (b & 7) * 4 + (b >> 6), g = (b >> 3) & 7, wg = p * 8 + g;
  TS(0);
  const rsrc_t ws = __builtin_amdgcn_make_buffer_rsrc(wsp, 0, WS_END, 0x00020000);
  const u32x4 sx = srd(x, 2048 * 512 * 2), sw = srd(wqkv, 512 * 1536 * 2);
  const unsigned t0 = g < 6 ? 11 * g : 66 + 10 * (g - 6), nt = g < 6 ? 11 : 10;

  const unsigned r8 = l >> 3, s8 = l & 7;
  unsigned vX[2], vW[2];
#pragma unroll
  for (int e = 0; e < 2; ++e) {
    vX[e] = (64 * p + 16 * w + 8 * e + r8) * 1024 + ((s8 ^ (4 * e + (r8 >> 1))) << 4);
    vW[e] = r8 * 3072 + g * 128 + (((((s8 >> 1) ^ ((r8 & 2) | e)) << 1) | (s8 & 1)) << 4);
  }
  auto stage = [&](int S) {
    const unsigned slot = L_RING + (S & 3) * 16384;
    if (S < 8) {
      if (S < 4) {
#pragma unroll
        for (int e = 0; e < 2; ++e) glds(sx, L_X + (2 * S) * 8192 + (2 * w + e) * 1024, vX[e], (2 * S) * 128);
      }
#pragma unroll
      for (int e = 0; e < 2; ++e)
#pragma unroll
        for (int sg = 0; sg < 2; ++sg) glds(sw, slot + sg * 8192 + (2 * w + e) * 1024, vW[sg], (S * 64 + 16 * w + 8 * e) * 3072 + (1 + sg) * 1024);
      if (S < 4) {
#pragma unroll
        for (int e = 0; e < 2; ++e) glds(sx, L_X + (2 * S + 1) * 8192 + (2 * w + e) * 1024, vX[e], (2 * S + 1) * 128);
      }
    } else {
#pragma unroll
      for (int e = 0; e < 4; ++e) glds(sw, slot + (4 * w + e) * 1024, vW[e & 1], ((S - 8) * 128 + 32 * w + 8 * e) * 3072);
    }
  };
  stage(0);
  const unsigned tks = w == 0 ? p : p + 32 * (w - 1);
  {
    const u32x4 st = w == 0 ? srd(wo, 512 * 512 * 2) : srd(w2, 1376 * 512 * 2);
#pragma unroll
    for (int e = 0; e < 2; ++e) glds(st, L_T0 + w * 2048 + e * 1024, r8 * 1024 + g * 128 + s8 * 16, ((tks < 86 ? tks : 85) * 16 + 8 * e) * 1024);
    const u32x4 s1 = srd(w1, 512 * 1376 * 2);
    const unsigned v1 = (l & 31) < 2 * nt ? (l >> 5) * 2752 + t0 * 32 + (l & 31) * 16 : OOB;
#pragma unroll
    for (int e = 0; e < 2; ++e) glds(s1, L_T1 + (2 * w + e) * 1024, v1, (16 * p + 4 * w + 2 * e) * 2752);
    glds(srd(wphi, 32 * 256 * 2), L_PHI + w * 1024, l < 32 ? l * 16 : OOB, (4 * g + w) * 512);
  }
  stage(1);
  stage(2);
  const u64 ep = *(const __attribute__((address_space(4))) u64*)(wsp + WS_EPOCH) + 1;
  const u32x2 tag = {(unsigned)ep, (unsigned)(ep >> 32)};

  const unsigned a4 = l >> 4, q4 = (l >> 2) & 3, p4 = l & 3, h = l >> 5, t5 = l & 31, t4 = l & 15;
  const unsigned aW = (a4 & 1) * 8192 + h * 1024 + q4 * 128 + ((w ^ ((q4 & 2) | (a4 & 1))) << 5) + ((((p4 & 1) << 1) | (p4 >> 1)) << 3);
  const unsigned aX = t5 * 128 + ((h ^ ((t5 >> 1) & 7)) << 4);
  const u32x4 ones = {0x3f803f80u, 0x3f803f80u, 0x3f803f80u, 0x3f803f80u};
  f32x16 acc[2] = {}, cs = {};
  float su[2] = {0.f, 0.f}, sq[2] = {0.f, 0.f};
  float mean5[2], rstd5[2], mean4[4];
#pragma unroll
  for (int S = 0; S < 8; ++S) {
    if (S == 0) VMW(21); else if (S == 1) VMW(16); else if (S == 2) VMW(12); else VMW(8);
    BAR();
    stage(S + 3);
    const unsigned slot = L_RING + (S & 3) * 16384;
#pragma unroll
    for (int ks = 0; ks < 4; ++ks) {
      u32x4 wf = trfrag(slot + aW + ks * 2048, slot + aW + ks * 2048 + 512);
#pragma unroll
      for (int tt = 0; tt < 2; ++tt) acc[tt] = mfma32(wf, lrd(L_X + S * 8192 + tt * 4096 + (aX ^ (ks * 32))), acc[tt]);
      cs = mfma32(wf, ones, cs);
    }
    if (S < 4) {
#pragma unroll
      for (int c = 0; c < 2; ++c)
#pragma unroll
        for (int tt = 0; tt < 2; ++tt) {
          u32x4 v = lrd(L_X + (2 * S + c) * 8192 + tt * 4096 + (aX ^ (w * 32)));
#pragma unroll
          for (int i = 0; i < 4; ++i) { su[tt] = dot2(v[i], 0x3f803f80u, su[tt]); sq[tt] = dot2(v[i], v[i], sq[tt]); }
        }
    }
    if (S == 1) {
#pragma unroll
      for (int ct = 0; ct < 2; ++ct) {
        const unsigned a = L_T0 + w * 2048 + (8 * h + q4) * 128 + ct * 64 + (a4 & 1) * 32 + p4 * 8;
        const unsigned dst = w == 0 ? WS_WO + ((g * 32 + tks) * 2 + ct) * 1024 : WS_W2 + ((g * 88 + tks) * 2 + ct) * 1024;
        st16(ws, trfrag(a, a + 512), tks < 86 ? l * 16 : OOB, dst);
      }
#pragma unroll
      for (int e = 0; e < 2; ++e) {
        const unsigned tl = 2 * (w + 4 * e) + h;
        const unsigned a = L_T1 + (8 * (a4 & 1) + q4) * 512 + tl * 32 + p4 * 8;
        st16(ws, trfrag(a, a + 2048), tl < nt ? (t0 + tl) * 16384 + t5 * 16 : OOB, WS_W1 + (p >> 1) * 1024 + (p & 1) * 512);
      }
    }
    if (S == 3) {
#pragma unroll
      for (int tt = 0; tt < 2; ++tt) *(LAS f32x2*)(L_ST + ((w * 2 + h) * 64 + tt * 32 + t5) * 8) = f32x2{su[tt], sq[tt]};
    }
    if (S == 4) {
      f32x2 a = {0.f, 0.f};
#pragma unroll
      for (int i = 0; i < 8; ++i) a += *(LAS f32x2*)(L_ST + (i * 64 + l) * 8);
      const float mean = a.x * (1.f / 512), var = fmaxf(a.y * (1.f / 512) - mean * mean, 0.f);
      *(LAS f32x2*)(L_MR + (w * 64 + l) * 8) = f32x2{mean, __builtin_amdgcn_rsqf(var + 1e-5f)};
    }
    if (S == 5) {
#pragma unroll
      for (int tt = 0; tt < 2; ++tt) { f32x2 a = *(LAS f32x2*)(L_MR + (w * 64 + tt * 32 + t5) * 8); mean5[tt] = a.x; rstd5[tt] = a.y; }
#pragma unroll
      for (int m = 0; m < 4; ++m) mean4[m] = *(LAS float*)(L_MR + (w * 64 + m * 16 + t4) * 8);
    }
  }
  TS(1);
  u32x4 pf;
  {
    const unsigned a = L_PHI + w * 1024 + (8 * h + q4) * 32 + (2 * (a4 & 1) + (p4 >> 1)) * 8;
    pf = trfrag(a, a + 128) ^ ((l & 4) ? 0x80008000u : 0u);
  }
  constexpr float LOG2E = 1.4426950408889634f;
  {
    f32x4 kv0 = {}, kv1 = {}, z0 = {}, z1 = {};
    const unsigned ePH = L_EP + w * 4096, eV = ePH + 2560;
#pragma unroll
    for (int tt = 0; tt < 2; ++tt) {
      float kk[16];
#pragma unroll
      for (int i = 0; i < 16; ++i) kk[i] = fmaf(-mean5[tt], cs[i], acc[tt][i]);
      f32x16 pp = mfma32(pf, u32x4{pk(kk[0], kk[1]), pk(kk[2], kk[3]), pk(kk[4], kk[5]), pk(kk[6], kk[7])}, f32x16{});
      const float rs = rstd5[tt];
      lwr(eV + t5 * 48 + h * 16, u32x4{pk(kk[8] * rs, kk[9] * rs), pk(kk[10] * rs, kk[11] * rs), pk(kk[12] * rs, kk[13] * rs), pk(kk[14] * rs, kk[15] * rs)});
      unsigned ph[8];
      softmax16(pp, rs * LOG2E, ph);
      lwr(ePH + t5 * 80 + h * 32, u32x4{ph[0], ph[1], ph[2], ph[3]});
      lwr(ePH + t5 * 80 + h * 32 + 16, u32x4{ph[4], ph[5], ph[6], ph[7]});
      const unsigned aP = ePH + (8 * a4 + q4) * 80 + p4 * 16, aV = eV + (8 * a4 + q4) * 48 + p4 * 8;
      u32x4 f0 = trfrag(aP, aP + 320), f1 = trfrag(aP + 8, aP + 328), fv = trfrag(aV, aV + 192);
      kv0 = mfma16(f0, fv, kv0); kv1 = mfma16(f1, fv, kv1);
      z0 = mfma16(f0, ones, z0); z1 = mfma16(f1, ones, z1);
    }
    VMW(0);
    BAR();
    stage(11);
    const unsigned dst = WS_KV + ((4 * g + w) * 32 + p) * 1152;
    st16(ws, u32x4{pk(kv0[0], kv0[1]), pk(kv0[2], kv0[3]), pk(kv1[0], kv1[1]), pk(kv1[2], kv1[3])}, l * 16, dst);
    st16(ws, u32x4{pk(z0[0], z0[1]), pk(z0[2], z0[3]), pk(z1[0], z1[1]), pk(z1[2], z1[3])}, t4 == 0 ? 1024 + a4 * 16 : OOB, dst);
    FENCE();
  }
  TS(2);
  u32x4 pq[4];
  u32x4 xr[2];
  {
    f32x4 aq[4] = {}, cq = {};
    const unsigned aWq = a4 * 1024 + q4 * 128 + ((w ^ ((q4 & 2) | (a4 & 1))) << 5) + p4 * 8;
    const unsigned aXq = t4 * 128 + ((a4 ^ (t4 >> 1)) << 4);
#pragma unroll
    for (int S = 8; S < 12; ++S) {
      if (S == 10) { VMW(0); BAR(); set_flag(ws, WS_FLAG + (1 * 256 + wg) * 128, tag, tid); TS(3); }
      const unsigned slot = L_RING + (S & 3) * 16384;
#pragma unroll
      for (int ks = 0; ks < 4; ++ks) {
        const int K2 = 4 * (S - 8) + ks;
        u32x4 wf = trfrag(slot + aWq + ks * 4096, slot + aWq + ks * 4096 + 512);
#pragma unroll
        for (int m = 0; m < 4; ++m) aq[m] = mfma16(wf, lrd(L_X + (K2 >> 1) * 8192 + m * 2048 + (aXq ^ ((K2 & 1) * 64))), aq[m]);
        cq = mfma16(wf, ones, cq);
      }
    }
#pragma unroll
    for (int tt = 0; tt < 2; ++tt) {
      const unsigned row = 32 * tt + 8 * w + s8;
      xr[tt] = lrd(L_X + g * 8192 + row * 128 + ((r8 ^ ((row >> 1) & 7)) << 4));
      unsigned qa[2][2];
#pragma unroll
      for (int e = 0; e < 2; ++e) {
        const f32x4 a = aq[2 * tt + e]; const float mu = mean4[2 * tt + e];
        qa[e][0] = pk(fmaf(-mu, cq[0], a[0]), fmaf(-mu, cq[1], a[1]));
        qa[e][1] = pk(fmaf(-mu, cq[2], a[2]), fmaf(-mu, cq[3], a[3]));
      }
      swap16(qa[0][0], qa[1][0]); swap16(qa[0][1], qa[1][1]);
      f32x16 pp = mfma32(pf, u32x4{qa[0][0], qa[0][1], qa[1][0], qa[1][1]}, f32x16{});
      unsigned ph[8];
      softmax16(pp, rstd5[tt] * LOG2E, ph);
#pragma unroll
      for (int i = 0; i < 4; ++i) swap16(ph[i], ph[4 + i]);
      pq[2 * tt] = u32x4{ph[0], ph[1], ph[2], ph[3]};
      pq[2 * tt + 1] = u32x4{ph[4], ph[5], ph[6], ph[7]};
    }
  }
  TS(4);
  if (w == 0) wait_flags(ws, t5 * 1024, WS_FLAG + (1 * 256 + g) * 128, tag);
  BAR();
  TS(5);
  {
    const unsigned src = WS_KV + (4 * g + w) * 32 * 1152;
    u32x4 zf[2], kp[32];
#pragma unroll
    for (int i = 0; i < 2; ++i) zf[i] = ld16(ws, t4 * 1152 + 1024 + a4 * 16, src + i * 16 * 1152);
#pragma unroll
    for (int i = 0; i < 32; ++i) kp[i] = ld16(ws, l * 16, src + i * 1152);
    FENCE();
    float inv[4];
#pragma unroll
    for (int m = 0; m < 4; ++m) {
      f32x4 d = mfma16(zf[1], pq[m], mfma16(zf[0], pq[m], f32x4{}));
      inv[m] = __builtin_amdgcn_rcpf(xsum32(xsum16((d[0] + d[1]) + (d[2] + d[3]))) + 1e-6f);
    }
    const unsigned one = (a4 == q4) ? ((p4 & 1) ? 0x3f800000u : 0x00003f80u) : 0u;
    const unsigned e0 = (p4 & 2) ? 0u : one, e1 = (p4 & 2) ? one : 0u;
    const u32x4 sel0 = {e0, e1, 0u, 0u}, sel1 = {0u, 0u, e0, e1};
    f32x4 k0 = {}, k1 = {};
#pragma unroll
    for (int i = 0; i < 32; ++i) { k0 = mfma16(sel0, kp[i], k0); k1 = mfma16(sel1, kp[i], k1); }
    const u32x4 kf = {pk(k0[0], k0[1]), pk(k0[2], k0[3]), pk(k1[0], k1[1]), pk(k1[2], k1[3])};
#pragma unroll
    for (int tt = 0; tt < 2; ++tt) {
      unsigned oa[2][2];
#pragma unroll
      for (int e = 0; e < 2; ++e) {
        const f32x4 n = mfma16(kf, pq[2 * tt + e], f32x4{}); const float iv = inv[2 * tt + e];
        oa[e][0] = pk(n[0] * iv, n[1] * iv); oa[e][1] = pk(n[2] * iv, n[3] * iv);
      }
      swap16(oa[0][0], oa[1][0]); swap16(oa[0][1], oa[1][1]);
      st16(ws, u32x4{oa[0][0], oa[0][1], oa[1][0], oa[1][1]}, l * 16, WS_O + ((p * 32 + 4 * g + w) * 2 + tt) * 1024);
    }
  }
  TS(6);
  VMW(0);
  BAR();
  set_flag(ws, WS_FLAG + (2 * 256 + wg) * 128, tag, tid);
  TS(7);
  u32x4 wof[16], w1f[3][16], w2f[44];
  unsigned sW1[3], vL[3];
  {
    const unsigned tb = t0 + ((nt == 10 && w == 3) ? 8 : 3 * w);
    const bool ok = w < (nt == 10 ? 2 : 3);
    sW1[0] = tb; sW1[1] = tb + 1; sW1[2] = ok ? tb + 2 : tb;
    vL[0] = vL[1] = l * 16; vL[2] = ok ? l * 16 : OOB;
  }
  const unsigned vT = w == 3 ? OOB : l * 16;
#pragma unroll
  for (int i = 0; i < 16; ++i) wof[i] = ld16(ws, l * 16, WS_WO + (g * 64 + w * 16 + i) * 1024);
#pragma unroll
  for (int i = 0; i < 16; ++i) w1f[0][i] = ld16(ws, l * 16, WS_W1 + sW1[0] * 16384 + i * 1024);
  FENCE();
  const unsigned vF = (l & 1) * 128, sF = WS_FLAG + (p * 8 + 2 * w) * 128;
  wait_flags(ws, vF, sF + 2 * 256 * 128, tag);
  TS(8);
  f32x16 ac[2][2] = {};
  unsigned hb[2][4];
  const unsigned aRr = L_R + r8 * 1152 + (8 * w + s8) * 16;
  {
    u32x4 of[16];
#pragma unroll
    for (int i = 0; i < 16; ++i) of[i] = ld16(ws, l * 16, WS_O + (p * 64 + w * 16 + i) * 1024);
    FENCE();
#pragma unroll
    for (int ks = 0; ks < 8; ++ks)
#pragma unroll
      for (int tt = 0; tt < 2; ++tt)
#pragma unroll
        for (int ct = 0; ct < 2; ++ct) ac[tt][ct] = mfma32(wof[2 * ks + ct], of[2 * ks + tt], ac[tt][ct]);
#pragma unroll
    for (int u = 0; u < 4; ++u)
#pragma unroll
      for (int q = 0; q < 4; ++q) {
        const f32x16& a = ac[u >> 1][u & 1];
        *(LAS f32x4*)(L_R + ((w * 4 + u) * 4 + q) * 1152 + l * 16) = f32x4{a[4 * q], a[4 * q + 1], a[4 * q + 2], a[4 * q + 3]};
      }
    BAR();
    TS(9);
    if (b == 0 && tid == 0) __builtin_amdgcn_raw_buffer_store_b64(tag, ws, WS_EPOCH, 0, 16);
    f32x2 stt;
#pragma unroll
    for (int tt = 0; tt < 2; ++tt) {
      f32x4 lo = {blo(xr[tt][0]), bhi(xr[tt][0]), blo(xr[tt][1]), bhi(xr[tt][1])}, hi = {blo(xr[tt][2]), bhi(xr[tt][2]), blo(xr[tt][3]), bhi(xr[tt][3])};
#pragma unroll
      for (int s = 0; s < 4; ++s) {
        lo += *(LAS f32x4*)(aRr + (s * 16 + tt * 8) * 1152);
        hi += *(LAS f32x4*)(aRr + (s * 16 + tt * 8) * 1152 + 512);
      }
      hb[tt][0] = pk(lo[0], lo[1]); hb[tt][1] = pk(lo[2], lo[3]); hb[tt][2] = pk(hi[0], hi[1]); hb[tt][3] = pk(hi[2], hi[3]);
      st16(ws, u32x4{hb[tt][0], hb[tt][1], hb[tt][2], hb[tt][3]}, (r8 >> 2) * 1024 + ((r8 & 3) * 16 + s8) * 16,
           WS_H + ((p * 4 + 2 * tt + (w >> 1)) * 16 + 2 * g) * 1024 + (w & 1) * 128);
      float a = 0.f, c = 0.f;
#pragma unroll
      for (int i = 0; i < 4; ++i) { a = dot2(hb[tt][i], 0x3f803f80u, a); c = dot2(hb[tt][i], hb[tt][i], c); }
      a = xsum32(xsum16(xsum8(a))); c = xsum32(xsum16(xsum8(c)));
      if (tt == 0 || r8 == 1) stt = f32x2{a, c};
    }
    __builtin_amdgcn_raw_buffer_store_b64(__builtin_bit_cast(u32x2, stt), ws, l < 16 ? l * 8 : OOB, WS_HS + wg * 512 + w * 128, 16);
  }
  TS(10);
  VMW(0);
  BAR();
  set_flag(ws, WS_FLAG + (3 * 256 + wg) * 128, tag, tid);
  TS(11);
  u32x2 b1r[3];
  {
    const rsrc_t rb1 = __builtin_amdgcn_make_buffer_rsrc((void*)b1, 0, 1376 * 2, 0x00020000);
#pragma unroll
    for (int t = 0; t < 3; ++t) b1r[t] = __builtin_amdgcn_raw_buffer_load_b64(rb1, a4 * 8, sW1[t] * 32, 0);
  }
#pragma unroll
  for (int t = 1; t < 3; ++t)
#pragma unroll
    for (int i = 0; i < 16; ++i) w1f[t][i] = ld16(ws, vL[t], WS_W1 + sW1[t] * 16384 + i * 1024);
  FENCE();
  wait_flags(ws, vF, sF + 3 * 256 * 128, tag);
  TS(12);
  {
    const u32x4 sws = srd(wsp, WS_END);
    glds_dev(sws, L_HS + w * 1024, l * 16, WS_HS + p * 4096 + w * 1024);
#pragma unroll
    for (int m = 0; m < 4; ++m)
#pragma unroll
      for (int e = 0; e < 4; ++e) glds_dev(sws, L_H + (m * 16 + 4 * w + e) * 1024, l * 16, WS_H + (p * 64 + m * 16 + 4 * w + e) * 1024);
    f32x4 c1[3] = {};
#pragma unroll
    for (int ks = 0; ks < 16; ++ks)
#pragma unroll
      for (int t = 0; t < 3; ++t) c1[t] = mfma16(w1f[t][ks], ones, c1[t]);
    asm volatile("" : "+v"(c1[0]), "+v"(c1[1]), "+v"(c1[2]));
    VMW(0);
    BAR();
    TS(13);
#pragma unroll
    for (int i = 0; i < 44; ++i) w2f[i] = ld16(ws, i >= 40 ? vT : l * 16, WS_W2 + (g * 176 + w * 44 + i) * 1024);
    FENCE();
    {
      f32x2 a = {0.f, 0.f};
      const unsigned sl = ((l >> 3) & 3) * 128 + (l >> 5) * 64 + (l & 7) * 8;
#pragma unroll
      for (int i = 0; i < 8; ++i) a += *(LAS f32x2*)(L_HS + i * 512 + sl);
      const float mean = a.x * (1.f / 512), var = fmaxf(a.y * (1.f / 512) - mean * mean, 0.f);
      const float rs = __builtin_amdgcn_rsqf(var + 1e-5f);
      *(LAS f32x2*)(L_MR + (w * 64 + l) * 8) = f32x2{-mean * rs, rs};
    }
    constexpr float K1 = -2.f * 0.7978845608028654f * LOG2E, K2 = K1 * 0.044715f;
#pragma unroll
    for (int tt = 0; tt < 2; ++tt) {
      unsigned hq[2][3][2];
#pragma unroll
      for (int e = 0; e < 2; ++e) {
        const int m = 2 * tt + e;
        f32x4 a[3] = {};
#pragma unroll
        for (int ks = 0; ks < 16; ++ks) {
          const u32x4 hf = lrd(L_H + (m * 16 + ks) * 1024 + l * 16);
#pragma unroll
          for (int t = 0; t < 3; ++t) a[t] = mfma16(w1f[t][ks], hf, a[t]);
        }
        const f32x2 mr = *(LAS f32x2*)(L_MR + (w * 64 + m * 16 + t4) * 8);
#pragma unroll
        for (int t = 0; t < 3; ++t) {
          const float bb[4] = {blo(b1r[t].x), bhi(b1r[t].x), blo(b1r[t].y), bhi(b1r[t].y)};
          float y[4];
#pragma unroll
          for (int i = 0; i < 4; ++i) {
            const float v = fmaf(a[t][i], mr.y, fmaf(mr.x, c1[t][i], bb[i]));
            y[i] = v * __builtin_amdgcn_rcpf(1.f + __builtin_amdgcn_exp2f(v * fmaf(v * v, K2, K1)));
          }
          hq[e][t][0] = pk(y[0], y[1]); hq[e][t][1] = pk(y[2], y[3]);
        }
      }
#pragma unroll
      for (int t = 0; t < 3; ++t) {
        swap16(hq[0][t][0], hq[1][t][0]); swap16(hq[0][t][1], hq[1][t][1]);
        st16(ws, u32x4{hq[0][t][0], hq[0][t][1], hq[1][t][0], hq[1][t][1]}, vL[t], WS_H1 + ((p * 2 + tt) * 88 + sW1[t]) * 1024);
      }
    }
  }
  TS(14);
  VMW(0);
  BAR();
  set_flag(ws, WS_FLAG + (4 * 256 + wg) * 128, tag, tid);
  TS(15);
  const rsrc_t rb2 = __builtin_amdgcn_make_buffer_rsrc((void*)b2, 0, 512 * 2, 0x00020000);
  const u32x4 b2r = __builtin_amdgcn_raw_buffer_load_b128(rb2, r8 * 16, g * 128, 0);
  FENCE();
  wait_flags(ws, vF, sF + 4 * 256 * 128, tag);
  TS(16);
  {
    u32x4 hf[2][22];
#pragma unroll
    for (int tt = 0; tt < 2; ++tt)
#pragma unroll
      for (int i = 0; i < 22; ++i) hf[tt][i] = ld16(ws, i >= 20 ? vT : l * 16, WS_H1 + ((p * 2 + tt) * 88 + w * 22 + i) * 1024);
    FENCE();
#pragma unroll
    for (int tt = 0; tt < 2; ++tt) {
      f32x16 a0 = {}, a1 = {};
#pragma unroll
      for (int i = 0; i < 22; ++i) { a0 = mfma32(w2f[2 * i], hf[tt][i], a0); a1 = mfma32(w2f[2 * i + 1], hf[tt][i], a1); }
#pragma unroll
      for (int q = 0; q < 4; ++q) {
        *(LAS f32x4*)(L_R + ((w * 4 + tt * 2) * 4 + q) * 1152 + l * 16) = f32x4{a0[4 * q], a0[4 * q + 1], a0[4 * q + 2], a0[4 * q + 3]};
        *(LAS f32x4*)(L_R + ((w * 4 + tt * 2 + 1) * 4 + q) * 1152 + l * 16) = f32x4{a1[4 * q], a1[4 * q + 1], a1[4 * q + 2], a1[4 * q + 3]};
      }
    }
    BAR();
    TS(17);
    const rsrc_t ro = __builtin_amdgcn_make_buffer_rsrc(out, 0, 2048 * 512 * 2, 0x00020000);
#pragma unroll
    for (int tt = 0; tt < 2; ++tt) {
      f32x4 lo = {blo(hb[tt][0]) + blo(b2r[0]), bhi(hb[tt][0]) + bhi(b2r[0]), blo(hb[tt][1]) + blo(b2r[1]), bhi(hb[tt][1]) + bhi(b2r[1])};
      f32x4 hi = {blo(hb[tt][2]) + blo(b2r[2]), bhi(hb[tt][2]) + bhi(b2r[2]), blo(hb[tt][3]) + blo(b2r[3]), bhi(hb[tt][3]) + bhi(b2r[3])};
#pragma unroll
      for (int s = 0; s < 4; ++s) {
        lo += *(LAS f32x4*)(aRr + (s * 16 + tt * 8) * 1152);
        hi += *(LAS f32x4*)(aRr + (s * 16 + tt * 8) * 1152 + 512);
      }
      __builtin_amdgcn_raw_buffer_store_b128(u32x4{pk(lo[0], lo[1]), pk(lo[2], lo[3]), pk(hi[0], hi[1]), pk(hi[2], hi[3])}, ro, s8 * 1024 + r8 * 16,
                                             (64 * p + 32 * tt + 8 * w) * 1024 + g * 128, 0);
    }
  }
  TS(18);
}

__global__ void k_first(char* p){ if(threadIdx.x==0) *(u64*)(p+WS_DBG)=__builtin_amdgcn_s_memrealtime(); }
__global__ void k_last(char* p){ u64 t=*(volatile u64*)(p+WS_DBG); while(__builtin_amdgcn_s_memrealtime()-t<6000ull) {} }
extern "C" void kernel_launch(void* const* d_in, const int* in_sizes, int n_in, void* d_out, int out_size, void* d_ws, size_t ws_size, hipStream_t stream) {
  (void)hipFuncSetAttribute((const void*)hh_block, hipFuncAttributeMaxDynamicSharedMemorySize, L_END);
  k_first<<<1,64,0,stream>>>((char*)d_ws);
  hh_block<<<256, 256, L_END, stream>>>((const u16*)d_in[0], (const u16*)d_in[1], (const u16*)d_in[2], (const u16*)d_in[3], (const u16*)d_in[5],
                                        (const u16*)d_in[6], (const u16*)d_in[7], (const u16*)d_in[8], (u16*)d_out, (char*)d_ws);
  k_last<<<1,64,0,stream>>>((char*)d_ws);
}
```

```cpp
#include <hip/hip_runtime.h>
__global__ __launch_bounds__(256, 1) void hh_block(const void* x, unsigned x_n, unsigned x_f, const void* wqkv, unsigned wqkv_n, unsigned wqkv_f, const void* ws, unsigned ws_n, unsigned ws_f, const void* wphi, unsigned wphi_n, unsigned wphi_f, const void* wo, unsigned wo_n, unsigned wo_f, const void* w2, unsigned w2_n, unsigned w2_f, const void* w1, unsigned w1_n, unsigned w1_f, const void* b1, unsigned b1_n, unsigned b1_f, const void* b2, unsigned b2_n, unsigned b2_f, const void* out, unsigned out_n, unsigned out_f) {
  unsigned tid = threadIdx.x;
  asm volatile(
    ".Lhh_start:\n"
    "s_load_dwordx16 s[8:23], %1, 0x0\n"
    "s_load_dwordx16 s[24:39], %1, 0x40\n"
    "s_load_dwordx8 s[40:47], %1, 0x80\n"
    "s_getpc_b64 s[60:61]\n"
    "s_mov_b32 s48, %2\n"
    "v_readfirstlane_b32 s49, v0\n"
    "v_and_b32 v1, 63, v0\n"
    "v_lshrrev_b32 v8, 3, v1\n"
    "v_and_b32 v9, 7, v1\n"
    "s_lshr_b32 s49, s49, 6\n"
    "s_bfe_u32 s50, s48, 0x20001\n"
    "s_lshr_b32 s52, s48, 5\n"
    "s_lshl3_add_u32 s50, s50, s52\n"
    "s_and_b32 s51, s48, 1\n"
    "s_bfe_u32 s52, s48, 0x20003\n"
    "s_lshl2_add_u32 s51, s51, s52\n"
    "v_lshrrev_b32 v2, 4, v1\n"
    "v_xor_b32 v2, v2, v9\n"
    "v_lshlrev_b32 v2, 4, v2\n"
    "v_lshl_or_b32 v50, v8, 10, v2\n"
    "v_xor_b32 v51, 64, v50\n"
    "v_add_u32 v51, 0x1c00, v51\n"
    "v_and_b32 v2, 16, v1\n"
    "v_lshlrev_b32 v2, 2, v2\n"
    "v_lshlrev_b32 v3, 4, v9\n"
    "v_xor_b32 v2, v2, v3\n"
    "v_mul_u32_u24 v3, 0xc00, v8\n"
    "v_or_b32 v56, v2, v3\n"
    "v_xor_b32 v54, 32, v56\n"
    "v_add_u32 v52, 0x400, v56\n"
    "v_add_u32 v53, 0x6000, v56\n"
    "v_add_u32 v55, 0x5c00, v54\n"
    "s_lshl_b32 s65, s49, 11\n"
    "s_lshl_b32 s66, s49, 12\n"
    "s_lshl2_add_u32 s59, s50, s49\n"
    "s_lshl_b32 s59, s59, 14\n"
    "s_add_u32 s68, s50, s51\n"
    "s_mul_i32 s68, s68, 4\n"
    "s_lshr_b32 s52, s50, 3\n"
    "s_mul_i32 s52, s52, 1\n"
    "s_add_u32 s68, s68, s52\n"
    "s_and_b32 s68, s68, 7\n"
    "s_lshl_b32 s52, s68, 7\n"
    "s_add_u32 s59, s59, s52\n"
    "s_mul_i32 s67, s49, 0xc000\n"
    "s_lshl_b32 s52, s51, 7\n"
    "s_add_u32 s67, s67, s52\n"
    "s_and_b32 s60, s60, 0xffffff00\n"
    "s_mov_b32 s62, .Lhh_end-.Lhh_start\n"
    "s_mov_b32 s63, 0x20000\n"
    "s_lshr_b32 s53, s48, 3\n"
    "s_lshl2_add_u32 s53, s53, s49\n"
    "s_lshl_b32 s53, s53, 8\n"
    "v_lshl_add_u32 v4, v1, 2, s53\n"
    "buffer_load_dword v10, v4, s[60:63], 0 offen\n"
    "s_add_u32 m0, s65, 0\n"
    "s_waitcnt lgkmcnt(0)\n"
    "buffer_load_dwordx4 v50, s[8:11], s59 offen lds\n"
    "buffer_load_dwordx4 v51, s[8:11], s59 offen offset:1024 lds\n"
    "s_add_u32 m0, s66, 0x10000\n"
    "s_and_b32 s64, s59, 0x380\n"
    "s_mul_i32 s64, s64, 0x600\n"
    "s_add_u32 s64, s64, s67\n"
    "buffer_load_dwordx4 v52, s[12:15], s64 offen lds\n"
    "buffer_load_dwordx4 v53, s[12:15], s64 offen offset:1024 lds\n"
    "buffer_load_dwordx4 v54, s[12:15], s64 offen offset:2048 lds\n"
    "buffer_load_dwordx4 v55, s[12:15], s64 offen offset:3072 lds\n"
    "s_add_u32 m0, s65, 0x2000\n"
    "s_add_u32 s59, s59, 0x80\n"
    "s_and_b32 s59, s59, 0xfffffbff\n"
    "buffer_load_dwordx4 v50, s[8:11], s59 offen lds\n"
    "buffer_load_dwordx4 v51, s[8:11], s59 offen offset:1024 lds\n"
    "v_lshlrev_b32 v14, 4, v1\n"
    "v_mov_b32 v20, 0x7ff00000\n"
    "v_and_b32 v76, 0x1f0, v14\n"
    "s_lshl2_add_u32 s76, s51, s49\n"
    "s_lshl_b32 s74, s76, 9\n"
    "s_lshl_b32 s75, s49, 10\n"
    "s_mov_b32 s54, 0x157ffc\n"
    "s_buffer_load_dword s58, s[32:35], 0x0\n"
    "s_buffer_load_dword s58, s[28:31], 0x0\n"
    "s_buffer_load_dword s58, s[24:27], 0x0\n"
    "s_buffer_load_dword s58, s[32:35], s54\n"
    "s_buffer_load_dword s58, s[28:31], s54\n"
    "s_lshl4_add_u32 s55, s50, s51\n"
    "s_mul_i32 s54, s55, 0x2c00\n"
    "s_add_u32 s54, s54, 0x962800\n"
    "s_buffer_load_dword s58, s[16:19], s54\n"
    "s_lshl_b32 s54, s55, 12\n"
    "s_add_u32 s54, s54, 0x722000\n"
    "s_buffer_load_dword s58, s[16:19], s54\n"
    "s_add_u32 s55, s55, s51\n"
    "s_lshl_b32 s54, s55, 12\n"
    "s_add_u32 s54, s54, 0x522000\n"
    "s_buffer_load_dword s58, s[16:19], s54\n"
    "s_lshl_b32 s54, s50, 9\n"
    "s_add_u32 s54, s54, s51\n"
    "s_lshl_b32 s54, s54, 7\n"
    "s_buffer_load_dword s58, s[44:47], s54\n"
    "s_lshl_b32 s54, s51, 7\n"
    "s_buffer_load_dword s58, s[40:43], s54\n"
    "s_mul_i32 s54, s51, 0x158\n"
    "s_mul_i32 s55, s49, 0x60\n"
    "s_add_u32 s54, s54, s55\n"
    "s_buffer_load_dword s58, s[36:39], s54\n"
    "s_load_dwordx2 s[56:57], s[16:17], 0x0\n"
    "s_mul_i32 s69, s51, 11\n"
    "s_add_u32 s69, s69, 1\n"
    "s_lshr_b32 s69, s69, 1\n"
    "s_min_u32 s69, s69, 38\n"
    "s_lshr_b32 s70, 21, s51\n"
    "s_and_b32 s70, s70, 1\n"
    "s_add_u32 s70, s70, 5\n"
    "s_lshl_b32 s52, s49, 5\n"
    "s_add_u32 s52, s52, s50\n"
    "s_sub_u32 s52, s52, 32\n"
    "s_cmp_eq_u32 s49, 0\n"
    "s_cselect_b32 s71, s50, s52\n"
    "s_cselect_b64 s[60:61], s[24:25], s[28:29]\n"
    "s_cselect_b64 s[62:63], s[26:27], s[30:31]\n"
    "s_min_u32 s52, s71, 0x55\n"
    "s_lshl_b32 s52, s52, 14\n"
    "s_xor_b32 s53, s51, 0\n"
    "s_lshl_b32 s53, s53, 7\n"
    "s_add_u32 s72, s52, s53\n"
    "s_add_u32 m0, s66, 0x14000\n"
    "s_and_b32 s64, s59, 0x380\n"
    "s_mul_i32 s64, s64, 0x600\n"
    "s_add_u32 s64, s64, s67\n"
    "buffer_load_dwordx4 v52, s[12:15], s64 offen lds\n"
    "v_lshlrev_b32 v32, 4, v9\n"
    "v_lshl_or_b32 v30, v8, 10, v32\n"
    "v_add_u32 v31, 0x1c00, v30\n"
    "v_lshrrev_b32 v21, 5, v1\n"
    "v_lshrrev_b32 v33, 2, v9\n"
    "v_lshl_add_u32 v33, s49, 1, v33\n"
    "v_cmp_gt_u32 vcc, s70, v33\n"
    "v_mul_u32_u24 v33, 0xac0, v8\n"
    "v_add_u32 v32, v32, v33\n"
    "s_mul_i32 s52, s50, 0xac00\n"
    "s_lshl1_add_u32 s53, s49, s69\n"
    "s_lshl_b32 s53, s53, 6\n"
    "s_add_u32 s73, s52, s53\n"
    "v_cndmask_b32 v32, v20, v32, vcc\n"
    "v_add_u32 v33, 0x5200, v32\n"
    "v_add_u32 v57, 0x5c00, v54\n"
    "buffer_load_dwordx4 v53, s[12:15], s64 offen offset:1024 lds\n"
    "v_add_u32 v58, 0xb800, v56\n"
    "v_add_u32 v59, 0xb800, v57\n"
    "v_bfe_u32 v28, v1, 2, 2\n"
    "v_and_b32 v77, 3, v1\n"
    "v_lshrrev_b32 v29, 4, v1\n"
    "v_and_b32 v78, 1, v29\n"
    "v_and_b32 v23, 15, v1\n"
    "v_and_or_b32 v2, v28, 2, v78\n"
    "v_xor_b32 v2, s49, v2\n"
    "v_lshlrev_b32 v2, 5, v2\n"
    "v_lshl_or_b32 v2, v28, 7, v2\n"
    "v_or_b32 v2, 0x10000, v2\n"
    "v_lshl_or_b32 v3, v77, 3, v2\n"
    "v_lshl_or_b32 v80, v29, 10, v3\n"
    "v_and_b32 v3, 1, v1\n"
    "buffer_load_dwordx4 v54, s[12:15], s64 offen offset:2048 lds\n"
    "v_lshl_or_b32 v2, v3, 4, v2\n"
    "v_bfe_u32 v3, v1, 1, 1\n"
    "v_lshl_or_b32 v2, v3, 3, v2\n"
    "v_lshl_or_b32 v2, v78, 11, v2\n"
    "v_lshl_or_b32 v79, v21, 10, v2\n"
    "v_bfe_u32 v2, v1, 1, 3\n"
    "v_xor_b32 v3, v2, v21\n"
    "v_lshlrev_b32 v3, 4, v3\n"
    "v_lshl_or_b32 v81, v76, 3, v3\n"
    "v_xor_b32 v82, 32, v81\n"
    "v_xor_b32 v83, 64, v81\n"
    "v_xor_b32 v84, 0x60, v81\n"
    "v_xor_b32 v3, v2, v29\n"
    "v_lshlrev_b32 v3, 4, v3\n"
    "v_lshl_or_b32 v85, v23, 7, v3\n"
    "v_xor_b32 v86, 64, v85\n"
    "buffer_load_dwordx4 v55, s[12:15], s64 offen offset:3072 lds\n"
    "v_add_u32 v87, s65, v85\n"
    "v_add_u32 v88, s65, v86\n"
    "v_mov_b32 v46, 0x3f803f80\n"
    "v_mov_b32 v47, 0x3f803f80\n"
    "v_mov_b32 v48, 0x3f803f80\n"
    "v_mov_b32 v49, 0x3f803f80\n"
    "v_xor_b32 v2, v28, v29\n"
    "v_and_b32 v2, 1, v2\n"
    "v_cmp_eq_u32 vcc, 0, v2\n"
    "s_nop 1\n"
    "v_cndmask_b32 v24, 0, v46, vcc\n"
    "v_cndmask_b32 v25, 0, v46, vcc\n"
    "v_cndmask_b32 v26, 0, v46, vcc\n"
    "v_cndmask_b32 v27, 0, v46, vcc\n"
    "v_lshl_or_b32 v38, v21, 3, v28\n"
    "v_lshlrev_b32 v44, 5, v38\n"
    "v_lshlrev_b32 v38, 7, v38\n"
    "s_add_u32 m0, s65, 0x4000\n"
    "s_add_u32 s59, s59, 0x80\n"
    "s_and_b32 s59, s59, 0xfffffbff\n"
    "buffer_load_dwordx4 v50, s[8:11], s59 offen lds\n"
    "v_lshl_or_b32 v38, v78, 5, v38\n"
    "s_add_u32 s52, s66, 0x20000\n"
    "v_add_u32 v38, s52, v38\n"
    "v_and_b32 v39, 24, v79\n"
    "v_or_b32 v39, v39, v38\n"
    "v_lshl_or_b32 v38, v77, 3, v38\n"
    "s_xor_b32 s53, s51, 0\n"
    "s_lshl_b32 s52, s53, 16\n"
    "s_add_u32 s52, s52, 0xca000\n"
    "s_mul_i32 s53, s53, 0x2c000\n"
    "s_add_u32 s53, s53, 0x14a000\n"
    "s_cmp_eq_u32 s49, 0\n"
    "s_cselect_b32 s52, s52, s53\n"
    "s_lshl_b32 s53, s71, 11\n"
    "s_add_u32 s89, s52, s53\n"
    "s_cmp_lt_u32 s71, 0x56\n"
    "s_cselect_b32 s53, 0, 0x7ff00000\n"
    "buffer_load_dwordx4 v51, s[8:11], s59 offen offset:1024 lds\n"
    "v_add_u32 v40, s53, v14\n"
    "s_lshl1_add_u32 s52, s49, 1\n"
    "s_cmp_lt_u32 s52, s70\n"
    "s_cselect_b32 s53, 0, 0x7ff00000\n"
    "s_or_b32 s53, s53, 0x8000\n"
    "v_add_u32 v43, s53, v14\n"
    "s_lshl1_add_u32 s52, s49, s69\n"
    "s_lshl_b32 s52, s52, 5\n"
    "s_add_u32 s52, s52, s50\n"
    "s_lshl_b32 s52, s52, 10\n"
    "s_add_u32 s90, s52, 0x2aa000\n"
    "v_lshl_or_b32 v44, v78, 4, v44\n"
    "v_bfe_u32 v45, v1, 1, 1\n"
    "v_lshl_or_b32 v44, v45, 3, v44\n"
    "s_add_u32 s52, s75, 0x24000\n"
    "v_add_u32 v44, s52, v44\n"
    "v_and_b32 v45, 1, v28\n"
    "s_add_u32 m0, s75, 0x24000\n"
    "s_nop 0\n"
    "buffer_load_dwordx4 v76, s[20:23], s74 offen lds\n"
    "v_mul_u32_u24 v45, 0x8000, v45\n"
    "v_lshl_or_b32 v45, v45, 16, v45\n"
    "v_mov_b32 v204, 0\n"
    "v_mov_b32 v205, 0\n"
    "v_mov_b32 v208, 0\n"
    "v_mov_b32 v209, 0\n"
    "v_mov_b32 v212, 0\n"
    "v_mov_b32 v213, 0\n"
    "v_mov_b32 v216, 0\n"
    "v_mov_b32 v217, 0\n"
    "v_mov_b32 v220, 0\n"
    "v_mov_b32 v221, 0\n"
    "v_mov_b32 v224, 0\n"
    "v_mov_b32 v225, 0\n"
    "v_mov_b32 v228, 0\n"
    "v_mov_b32 v229, 0\n"
    "s_add_u32 m0, s66, 0x18000\n"
    "s_and_b32 s64, s59, 0x380\n"
    "s_mul_i32 s64, s64, 0x600\n"
    "s_add_u32 s64, s64, s67\n"
    "buffer_load_dwordx4 v52, s[12:15], s64 offen lds\n"
    "s_lshl_b32 s52, s49, 7\n"
    "s_add_u32 s52, s52, 0x25000\n"
    "v_lshl_add_u32 v34, v23, 3, s52\n"
    "v_cmp_eq_u32 vcc, v28, v29\n"
    "v_add_u32 v35, 0x200, v34\n"
    "s_nop 0\n"
    "v_cndmask_b32 v34, v35, v34, vcc\n"
    "v_lshrrev_b32 v35, 1, v76\n"
    "v_add_u32 v35, 0x25000, v35\n"
    "v_lshlrev_b32 v36, 3, v23\n"
    "v_add_u32 v36, 0x25000, v36\n"
    "s_add_u32 s52, s66, 0x20000\n"
    "s_add_u32 s53, s66, 0x20a00\n"
    "v_mad_u32_u24 v89, v76, 5, s52\n"
    "v_lshl_add_u32 v89, v21, 5, v89\n"
    "v_mad_u32_u24 v242, v76, 3, s53\n"
    "buffer_load_dwordx4 v53, s[12:15], s64 offen offset:1024 lds\n"
    "v_lshl_add_u32 v242, v21, 4, v242\n"
    "v_lshl_or_b32 v2, v29, 3, v28\n"
    "v_lshlrev_b32 v2, 4, v2\n"
    "v_mad_u32_u24 v243, v2, 5, s52\n"
    "v_lshl_add_u32 v243, v77, 4, v243\n"
    "v_mad_u32_u24 v244, v2, 3, s53\n"
    "v_lshl_add_u32 v244, v77, 3, v244\n"
    "s_lshl3_add_u32 s81, s50, s51\n"
    "v_cmp_gt_u32 vcc, 16, v1\n"
    "v_lshlrev_b32 v18, 3, v1\n"
    "s_nop 0\n"
    "v_cndmask_b32 v18, v20, v18, vcc\n"
    "s_lshl2_add_u32 s86, s81, s49\n"
    "s_lshl_b32 s86, s86, 7\n"
    "s_add_u32 s78, s86, 0xa000\n"
    "s_add_u32 s86, s86, 0xaa000\n"
    "s_lshl_b32 s77, s50, 12\n"
    "buffer_load_dwordx4 v54, s[12:15], s64 offen offset:2048 lds\n"
    "s_lshr_b32 s52, s51, 1\n"
    "s_lshl_b32 s52, s52, 7\n"
    "s_add_u32 s77, s77, s52\n"
    "s_and_b32 s52, s51, 1\n"
    "s_lshl3_add_u32 s77, s52, s77\n"
    "s_add_u32 s77, s77, 0xa000\n"
    "v_lshlrev_b32 v19, 8, v76\n"
    "v_lshl_add_u32 v19, s76, 7, v19\n"
    "v_mul_u32_u24 v15, 0, v21\n"
    "v_xor_b32 v19, v19, v15\n"
    "v_and_b32 v15, 8, v1\n"
    "s_lshl_b32 s83, s50, 5\n"
    "s_add_u32 s83, s83, s76\n"
    "s_lshl_b32 s83, s83, 11\n"
    "s_add_u32 s83, s83, 0x522000\n"
    "v_mov_b32 v90, -1\n"
    "buffer_load_dwordx4 v55, s[12:15], s64 offen offset:3072 lds\n"
    "v_mov_b32 v91, -1\n"
    "v_mov_b32 v92, -1\n"
    "v_mov_b32 v93, -1\n"
    "s_add_u32 s79, s69, s49\n"
    "s_lshr_b32 s52, s49, 1\n"
    "s_add_u32 s52, s52, 4\n"
    "s_add_u32 s80, s69, s52\n"
    "s_cmp_lt_u32 s52, s70\n"
    "s_cselect_b32 s52, 0, 0x7ff00000\n"
    "v_or_b32 v16, s52, v14\n"
    "s_and_b32 s88, s49, 1\n"
    "s_lshl_b32 s52, s88, 8\n"
    "v_xor_b32 v22, s52, v16\n"
    "s_cmp_eq_u32 s49, 3\n"
    "s_cselect_b32 s52, 0x7ff00000, 0\n"
    "v_or_b32 v17, s52, v14\n"
    "s_mul_i32 s52, s50, 0x2c000\n"
    "s_add_u32 m0, s65, 0x6000\n"
    "s_add_u32 s59, s59, 0x80\n"
    "s_and_b32 s59, s59, 0xfffffbff\n"
    "buffer_load_dwordx4 v50, s[8:11], s59 offen lds\n"
    "s_add_u32 s52, s52, 0x962000\n"
    "s_lshl_b32 s84, s79, 11\n"
    "s_add_u32 s84, s84, s52\n"
    "s_lshl1_add_u32 s85, s80, s88\n"
    "s_lshl_b32 s85, s85, 10\n"
    "s_add_u32 s85, s85, s52\n"
    "s_lshl_b32 s54, s76, 5\n"
    "s_add_u32 s54, s54, s50\n"
    "s_mul_i32 s54, s54, 0x480\n"
    "s_add_u32 s54, s54, 0x402000\n"
    "v_cmp_ge_u32 vcc, 1, v23\n"
    "v_mad_u32_u24 v7, v23, 63, v1\n"
    "v_mov_b32 v2, 0x3b000000\n"
    "v_cndmask_b32 v7, v20, v7, vcc\n"
    "s_and_b32 s52, s50, 3\n"
    "s_cmp_eq_u32 s52, 0\n"
    "s_cselect_b32 s52, 0, 0x7ff00000\n"
    "buffer_load_dwordx4 v51, s[8:11], s59 offen offset:1024 lds\n"
    "s_cmp_eq_u32 s50, 1\n"
    "s_cselect_b32 s52, 0x3e0, s52\n"
    "v_cmp_gt_u32 vcc, 8, v1\n"
    "v_add_u32 v41, s52, v14\n"
    "s_nop 0\n"
    "v_cndmask_b32 v41, v20, v41, vcc\n"
    "s_mul_i32 s87, s76, 0x480\n"
    "s_lshl_b32 s52, s50, 5\n"
    "s_add_u32 s87, s87, s52\n"
    "s_add_u32 s87, s87, 0x1000\n"
    "s_cmp_lt_u32 s50, 2\n"
    "s_cselect_b32 s52, 33, 49\n"
    "v_and_b32 v3, s52, v1\n"
    "v_cmp_eq_u32 vcc, 0, v3\n"
    "v_lshlrev_b32 v3, 1, v23\n"
    "v_lshl_or_b32 v3, v78, 10, v3\n"
    "v_cndmask_b32 v245, v20, v3, vcc\n"
    "v_cmp_eq_u32 vcc, 0, v77\n"
    "s_nop 1\n"
    "v_cndmask_b32 v246, 0, v2, vcc\n"
    "s_add_u32 m0, s66, 0x20000\n"
    "s_nop 0\n"
    "buffer_load_dwordx4 v30, s[60:63], s72 offen lds\n"
    "v_cmp_eq_u32 vcc, 1, v77\n"
    "s_nop 1\n"
    "v_cndmask_b32 v247, 0, v2, vcc\n"
    "v_cmp_eq_u32 vcc, 2, v77\n"
    "s_nop 1\n"
    "v_cndmask_b32 v248, 0, v2, vcc\n"
    "v_cmp_eq_u32 vcc, 3, v77\n"
    "s_nop 1\n"
    "v_cndmask_b32 v249, 0, v2, vcc\n"
    "v_bfe_u32 v4, v1, 1, 4\n"
    "v_lshlrev_b32 v3, 2, v21\n"
    "v_sub_u32 v4, v4, v3\n"
    "v_lshlrev_b32 v4, v4, 1\n"
    "v_and_b32 v3, 1, v1\n"
    "v_lshlrev_b32 v3, 4, v3\n"
    "v_mov_b32 v2, 0x3f80\n"
    "v_lshlrev_b32 v3, v3, v2\n"
    "v_bfe_i32 v60, v4, 0, 1\n"
    "buffer_load_dwordx4 v31, s[60:63], s72 offen offset:1024 lds\n"
    "v_bfe_i32 v61, v4, 1, 1\n"
    "v_bfe_i32 v62, v4, 2, 1\n"
    "v_bfe_i32 v63, v4, 3, 1\n"
    "v_bfe_i32 v64, v4, 8, 1\n"
    "v_bfe_i32 v65, v4, 9, 1\n"
    "v_bfe_i32 v66, v4, 10, 1\n"
    "v_bfe_i32 v67, v4, 11, 1\n"
    "v_and_b32 v60, v60, v3\n"
    "v_and_b32 v61, v61, v3\n"
    "v_and_b32 v62, v62, v3\n"
    "v_and_b32 v63, v63, v3\n"
    "v_and_b32 v64, v64, v3\n"
    "v_and_b32 v65, v65, v3\n"
    "v_and_b32 v66, v66, v3\n"
    "v_and_b32 v67, v67, v3\n"
    "v_accvgpr_write_b32 a184, v60\n"
    "s_add_u32 m0, s66, 0x20800\n"
    "s_nop 0\n"
    "buffer_load_dwordx4 v32, s[32:35], s73 offen lds\n"
    "v_accvgpr_write_b32 a185, v61\n"
    "v_accvgpr_write_b32 a186, v62\n"
    "v_accvgpr_write_b32 a187, v63\n"
    "v_accvgpr_write_b32 a188, v64\n"
    "v_accvgpr_write_b32 a189, v65\n"
    "v_accvgpr_write_b32 a190, v66\n"
    "v_accvgpr_write_b32 a191, v67\n"
    "s_mul_i32 s55, s76, 0x9000\n"
    "s_lshl_b32 s52, s50, 5\n"
    "s_add_u32 s55, s55, s52\n"
    "s_add_u32 s55, s55, 0x402000\n"
    "s_cmp_lt_u32 s50, 2\n"
    "s_cselect_b32 s82, 0, 0x7ff00000\n"
    "v_mul_u32_u24 v5, 0x48, v76\n"
    "v_lshl_or_b32 v5, v21, 4, v5\n"
    "v_or_b32 v6, s82, v5\n"
    "buffer_load_dwordx4 v33, s[32:35], s73 offen offset:1024 lds\n"
    "s_waitcnt vmcnt(17) lgkmcnt(0)\n"
    "s_barrier\n"
    "ds_read_b64_tr_b16 v[138:139], v79\n"
    "ds_read_b64_tr_b16 v[140:141], v79 offset:512\n"
    "ds_read_b128 v[154:157], v81\n"
    "ds_read_b128 v[158:161], v81 offset:4096\n"
    "ds_read_b64_tr_b16 v[142:143], v79 offset:4096\n"
    "ds_read_b64_tr_b16 v[144:145], v79 offset:4608\n"
    "s_add_u32 m0, s66, 0x1c000\n"
    "s_and_b32 s64, s59, 0x380\n"
    "s_mul_i32 s64, s64, 0x600\n"
    "s_add_u32 s64, s64, s67\n"
    "buffer_load_dwordx4 v52, s[12:15], s64 offen lds\n"
    "ds_read_b128 v[162:165], v82\n"
    "ds_read_b128 v[166:169], v82 offset:4096\n"
    "ds_read_b128 v[194:197], v87\n"
    "ds_read_b128 v[198:201], v88\n"
    "ds_read_b128 v[186:189], v87 offset:8192\n"
    "buffer_load_dwordx4 v53, s[12:15], s64 offen offset:1024 lds\n"
    "ds_read_b128 v[190:193], v88 offset:8192\n"
    "ds_read_b64_tr_b16 v[146:147], v79 offset:8192\n"
    "ds_read_b64_tr_b16 v[148:149], v79 offset:8704\n"
    "ds_read_b128 v[170:173], v83\n"
    "ds_read_b128 v[174:177], v83 offset:4096\n"
    "buffer_load_dwordx4 v54, s[12:15], s64 offen offset:2048 lds\n"
    "ds_read_b64_tr_b16 v[150:151], v79 offset:12288\n"
    "ds_read_b64_tr_b16 v[152:153], v79 offset:12800\n"
    "ds_read_b128 v[178:181], v84\n"
    "ds_read_b128 v[182:185], v84 offset:4096\n"
    "s_add_u32 s56, s56, 1\n"
    "s_addc_u32 s57, s57, 0\n"
    "buffer_load_dwordx4 v55, s[12:15], s64 offen offset:3072 lds\n"
    "v_mov_b32 v12, s56\n"
    "v_mov_b32 v13, s57\n"
    "s_waitcnt lgkmcnt(15)\n"
    "v_mfma_f32_16x16x32_bf16 v[126:129], v[24:27], v[138:141], 0\n"
    "v_mfma_f32_32x32x16_bf16 v[94:109], v[138:141], v[154:157], 0\n"
    "v_mfma_f32_32x32x16_bf16 v[110:125], v[138:141], v[158:161], 0\n"
    "s_add_u32 m0, s65, 0x8000\n"
    "s_add_u32 s59, s59, 0x80\n"
    "s_and_b32 s59, s59, 0xfffffbff\n"
    "buffer_load_dwordx4 v50, s[8:11], s59 offen lds\n"
    "s_waitcnt lgkmcnt(9)\n"
    "v_mfma_f32_16x16x32_bf16 v[130:133], v[46:49], v[186:189], 0\n"
    "v_mfma_f32_16x16x32_bf16 v[134:137], v[186:189], v[186:189], 0\n"
    "s_waitcnt lgkmcnt(8)\n"
    "v_mfma_f32_16x16x32_bf16 v[130:133], v[46:49], v[190:193], v[130:133]\n"
    "v_mfma_f32_16x16x32_bf16 v[134:137], v[190:193], v[190:193], v[134:137]\n"
    "v_mfma_f32_16x16x32_bf16 v[130:133], v[46:49], v[194:197], v[130:133]\n"
    "buffer_load_dwordx4 v51, s[8:11], s59 offen offset:1024 lds\n"
    "v_mfma_f32_16x16x32_bf16 v[134:137], v[194:197], v[194:197], v[134:137]\n"
    "v_mfma_f32_16x16x32_bf16 v[130:133], v[46:49], v[198:201], v[130:133]\n"
    "v_mfma_f32_16x16x32_bf16 v[134:137], v[198:201], v[198:201], v[134:137]\n"
    "v_mfma_f32_16x16x32_bf16 v[126:129], v[24:27], v[142:145], v[126:129]\n"
    "v_mfma_f32_32x32x16_bf16 v[94:109], v[142:145], v[162:165], v[94:109]\n"
    "v_mfma_f32_32x32x16_bf16 v[110:125], v[142:145], v[166:169], v[110:125]\n"
    "s_waitcnt vmcnt(17) lgkmcnt(0)\n"
    "s_barrier\n"
    "ds_read_b64_tr_b16 v[138:139], v79 offset:16384\n"
    "ds_read_b64_tr_b16 v[140:141], v79 offset:16896\n"
    "v_mfma_f32_16x16x32_bf16 v[126:129], v[24:27], v[146:149], v[126:129]\n"
    "ds_read_b128 v[154:157], v81 offset:8192\n"
    "v_mfma_f32_32x32x16_bf16 v[94:109], v[146:149], v[170:173], v[94:109]\n"
    "s_add_u32 m0, s66, 0x10000\n"
    "s_and_b32 s64, s59, 0x380\n"
    "s_mul_i32 s64, s64, 0x600\n"
    "s_add_u32 s64, s64, s67\n"
    "buffer_load_dwordx4 v52, s[12:15], s64 offen lds\n"
    "ds_read_b128 v[158:161], v81 offset:12288\n"
    "ds_read_b64_tr_b16 v[142:143], v79 offset:20480\n"
    "ds_read_b64_tr_b16 v[144:145], v79 offset:20992\n"
    "ds_read_b128 v[162:165], v82 offset:8192\n"
    "v_mfma_f32_32x32x16_bf16 v[110:125], v[146:149], v[174:177], v[110:125]\n"
    "buffer_load_dwordx4 v53, s[12:15], s64 offen offset:1024 lds\n"
    "ds_read_b128 v[166:169], v82 offset:12288\n"
    "ds_read_b128 v[186:189], v87 offset:16384\n"
    "ds_read_b128 v[190:193], v88 offset:16384\n"
    "v_mfma_f32_16x16x32_bf16 v[126:129], v[24:27], v[150:153], v[126:129]\n"
    "buffer_load_dwordx4 v54, s[12:15], s64 offen offset:2048 lds\n"
    "ds_read_b64_tr_b16 v[146:147], v79 offset:24576\n"
    "v_mfma_f32_32x32x16_bf16 v[94:109], v[150:153], v[178:181], v[94:109]\n"
    "ds_read_b64_tr_b16 v[148:149], v79 offset:25088\n"
    "ds_read_b128 v[170:173], v83 offset:8192\n"
    "ds_read_b128 v[174:177], v83 offset:12288\n"
    "buffer_load_dwordx4 v55, s[12:15], s64 offen offset:3072 lds\n"
    "v_mfma_f32_32x32x16_bf16 v[110:125], v[150:153], v[182:185], v[110:125]\n"
    "s_waitcnt lgkmcnt(12)\n"
    "v_mfma_f32_16x16x32_bf16 v[126:129], v[24:27], v[138:141], v[126:129]\n"
    "ds_read_b64_tr_b16 v[150:151], v79 offset:28672\n"
    "s_waitcnt lgkmcnt(12)\n"
    "v_mfma_f32_32x32x16_bf16 v[94:109], v[138:141], v[154:157], v[94:109]\n"
    "ds_read_b64_tr_b16 v[152:153], v79 offset:29184\n"
    "s_add_u32 m0, s65, 0xa000\n"
    "s_add_u32 s59, s59, 0x80\n"
    "s_and_b32 s59, s59, 0xfffffbff\n"
    "buffer_load_dwordx4 v50, s[8:11], s59 offen lds\n"
    "ds_read_b128 v[178:181], v84 offset:8192\n"
    "ds_read_b128 v[182:185], v84 offset:12288\n"
    "s_waitcnt lgkmcnt(14)\n"
    "v_mfma_f32_32x32x16_bf16 v[110:125], v[138:141], v[158:161], v[110:125]\n"
    "s_waitcnt lgkmcnt(9)\n"
    "v_mfma_f32_16x16x32_bf16 v[130:133], v[46:49], v[186:189], v[130:133]\n"
    "buffer_load_dwordx4 v51, s[8:11], s59 offen offset:1024 lds\n"
    "v_mfma_f32_16x16x32_bf16 v[134:137], v[186:189], v[186:189], v[134:137]\n"
    "s_waitcnt lgkmcnt(8)\n"
    "v_mfma_f32_16x16x32_bf16 v[130:133], v[46:49], v[190:193], v[130:133]\n"
    "v_mfma_f32_16x16x32_bf16 v[134:137], v[190:193], v[190:193], v[134:137]\n"
    "v_mfma_f32_16x16x32_bf16 v[126:129], v[24:27], v[142:145], v[126:129]\n"
    "v_mfma_f32_32x32x16_bf16 v[94:109], v[142:145], v[162:165], v[94:109]\n"
    "v_mfma_f32_32x32x16_bf16 v[110:125], v[142:145], v[166:169], v[110:125]\n"
    "s_waitcnt vmcnt(16) lgkmcnt(0)\n"
    "s_barrier\n"
    "ds_read_b64_tr_b16 v[138:139], v79 offset:32768\n"
    "ds_read_b64_tr_b16 v[140:141], v79 offset:33280\n"
    "v_mfma_f32_16x16x32_bf16 v[126:129], v[24:27], v[146:149], v[126:129]\n"
    "ds_read_b128 v[154:157], v81 offset:16384\n"
    "v_mfma_f32_32x32x16_bf16 v[94:109], v[146:149], v[170:173], v[94:109]\n"
    "s_add_u32 m0, s66, 0x14000\n"
    "s_and_b32 s64, s59, 0x380\n"
    "s_mul_i32 s64, s64, 0x600\n"
    "s_add_u32 s64, s64, s67\n"
    "buffer_load_dwordx4 v52, s[12:15], s64 offen lds\n"
    "ds_read_b128 v[158:161], v81 offset:20480\n"
    "ds_read_b64_tr_b16 v[142:143], v79 offset:36864\n"
    "ds_read_b64_tr_b16 v[144:145], v79 offset:37376\n"
    "ds_read_b128 v[162:165], v82 offset:16384\n"
    "v_mfma_f32_32x32x16_bf16 v[110:125], v[146:149], v[174:177], v[110:125]\n"
    "buffer_load_dwordx4 v53, s[12:15], s64 offen offset:1024 lds\n"
    "ds_read_b128 v[166:169], v82 offset:20480\n"
    "ds_read_b128 v[186:189], v87 offset:24576\n"
    "ds_read_b128 v[190:193], v88 offset:24576\n"
    "v_mfma_f32_16x16x32_bf16 v[126:129], v[24:27], v[150:153], v[126:129]\n"
    "ds_read_b64_tr_b16 v[146:147], v79 offset:40960\n"
    "buffer_load_dwordx4 v54, s[12:15], s64 offen offset:2048 lds\n"
    "v_mfma_f32_32x32x16_bf16 v[94:109], v[150:153], v[178:181], v[94:109]\n"
    "ds_read_b64_tr_b16 v[148:149], v79 offset:41472\n"
    "ds_read_b128 v[170:173], v83 offset:16384\n"
    "ds_read_b128 v[174:177], v83 offset:20480\n"
    "v_mfma_f32_32x32x16_bf16 v[110:125], v[150:153], v[182:185], v[110:125]\n"
    "buffer_load_dwordx4 v55, s[12:15], s64 offen offset:3072 lds\n"
    "s_waitcnt lgkmcnt(12)\n"
    "v_mfma_f32_16x16x32_bf16 v[126:129], v[24:27], v[138:141], v[126:129]\n"
    "ds_read_b64_tr_b16 v[150:151], v79 offset:45056\n"
    "s_waitcnt lgkmcnt(12)\n"
    "v_mfma_f32_32x32x16_bf16 v[94:109], v[138:141], v[154:157], v[94:109]\n"
    "ds_read_b64_tr_b16 v[152:153], v79 offset:45568\n"
    "ds_read_b128 v[178:181], v84 offset:16384\n"
    "s_add_u32 m0, s65, 0xc000\n"
    "s_add_u32 s59, s59, 0x80\n"
    "s_and_b32 s59, s59, 0xfffffbff\n"
    "buffer_load_dwordx4 v50, s[8:11], s59 offen lds\n"
    "ds_read_b128 v[182:185], v84 offset:20480\n"
    "ds_read_b64_tr_b16 v[238:239], v44\n"
    "s_waitcnt lgkmcnt(15)\n"
    "v_mfma_f32_32x32x16_bf16 v[110:125], v[138:141], v[158:161], v[110:125]\n"
    "ds_read_b64_tr_b16 v[240:241], v44 offset:128\n"
    "s_waitcnt lgkmcnt(11)\n"
    "v_mfma_f32_16x16x32_bf16 v[130:133], v[46:49], v[186:189], v[130:133]\n"
    "buffer_load_dwordx4 v51, s[8:11], s59 offen offset:1024 lds\n"
    "v_mfma_f32_16x16x32_bf16 v[134:137], v[186:189], v[186:189], v[134:137]\n"
    "s_waitcnt lgkmcnt(10)\n"
    "v_mfma_f32_16x16x32_bf16 v[130:133], v[46:49], v[190:193], v[130:133]\n"
    "v_mfma_f32_16x16x32_bf16 v[134:137], v[190:193], v[190:193], v[134:137]\n"
    "v_mfma_f32_16x16x32_bf16 v[126:129], v[24:27], v[142:145], v[126:129]\n"
    "v_mfma_f32_32x32x16_bf16 v[94:109], v[142:145], v[162:165], v[94:109]\n"
    "v_mfma_f32_32x32x16_bf16 v[110:125], v[142:145], v[166:169], v[110:125]\n"
    "s_waitcnt vmcnt(12) lgkmcnt(0)\n"
    "s_barrier\n"
    "ds_read_b64_tr_b16 v[138:139], v79 offset:49152\n"
    "ds_read_b64_tr_b16 v[140:141], v79 offset:49664\n"
    "v_mfma_f32_16x16x32_bf16 v[126:129], v[24:27], v[146:149], v[126:129]\n"
    "ds_read_b128 v[154:157], v81 offset:24576\n"
    "v_mfma_f32_32x32x16_bf16 v[94:109], v[146:149], v[170:173], v[94:109]\n"
    "ds_read_b128 v[158:161], v81 offset:28672\n"
    "s_add_u32 m0, s66, 0x18000\n"
    "s_and_b32 s64, s59, 0x380\n"
    "s_mul_i32 s64, s64, 0x600\n"
    "s_add_u32 s64, s64, s67\n"
    "buffer_load_dwordx4 v52, s[12:15], s64 offen lds\n"
    "ds_read_b64_tr_b16 v[142:143], v79 offset:53248\n"
    "ds_read_b64_tr_b16 v[144:145], v79 offset:53760\n"
    "ds_read_b128 v[162:165], v82 offset:24576\n"
    "v_mfma_f32_32x32x16_bf16 v[110:125], v[146:149], v[174:177], v[110:125]\n"
    "ds_read_b128 v[166:169], v82 offset:28672\n"
    "ds_read_b128 v[186:189], v87 offset:32768\n"
    "buffer_load_dwordx4 v53, s[12:15], s64 offen offset:1024 lds\n"
    "ds_read_b128 v[190:193], v88 offset:32768\n"
    "v_mfma_f32_16x16x32_bf16 v[126:129], v[24:27], v[150:153], v[126:129]\n"
    "ds_read_b64_tr_b16 v[146:147], v79 offset:57344\n"
    "v_mfma_f32_32x32x16_bf16 v[94:109], v[150:153], v[178:181], v[94:109]\n"
    "ds_read_b64_tr_b16 v[148:149], v79 offset:57856\n"
    "ds_read_b128 v[170:173], v83 offset:24576\n"
    "buffer_load_dwordx4 v54, s[12:15], s64 offen offset:2048 lds\n"
    "ds_read_b128 v[174:177], v83 offset:28672\n"
    "v_mfma_f32_32x32x16_bf16 v[110:125], v[150:153], v[182:185], v[110:125]\n"
    "s_waitcnt lgkmcnt(12)\n"
    "v_mfma_f32_16x16x32_bf16 v[126:129], v[24:27], v[138:141], v[126:129]\n"
    "ds_read_b64_tr_b16 v[150:151], v79 offset:61440\n"
    "s_waitcnt lgkmcnt(12)\n"
    "v_mfma_f32_32x32x16_bf16 v[94:109], v[138:141], v[154:157], v[94:109]\n"
    "ds_read_b64_tr_b16 v[152:153], v79 offset:61952\n"
    "buffer_load_dwordx4 v55, s[12:15], s64 offen offset:3072 lds\n"
    "ds_read_b128 v[178:181], v84 offset:24576\n"
    "ds_read_b128 v[182:185], v84 offset:28672\n"
    "s_waitcnt lgkmcnt(14)\n"
    "v_mfma_f32_32x32x16_bf16 v[110:125], v[138:141], v[158:161], v[110:125]\n"
    "ds_read_b64_tr_b16 v[60:61], v38\n"
    "ds_read_b64_tr_b16 v[62:63], v38 offset:512\n"
    "s_add_u32 m0, s65, 0xe000\n"
    "s_add_u32 s59, s59, 0x80\n"
    "s_and_b32 s59, s59, 0xfffffbff\n"
    "buffer_load_dwordx4 v50, s[8:11], s59 offen lds\n"
    "ds_read_b64_tr_b16 v[64:65], v38 offset:64\n"
    "ds_read_b64_tr_b16 v[66:67], v38 offset:576\n"
    "s_waitcnt lgkmcnt(13)\n"
    "v_mfma_f32_16x16x32_bf16 v[130:133], v[46:49], v[186:189], v[130:133]\n"
    "ds_read_b64_tr_b16 v[68:69], v39 offset:2048\n"
    "v_mfma_f32_16x16x32_bf16 v[134:137], v[186:189], v[186:189], v[134:137]\n"
    "ds_read_b64_tr_b16 v[70:71], v39 offset:2560\n"
    "buffer_load_dwordx4 v51, s[8:11], s59 offen offset:1024 lds\n"
    "s_waitcnt lgkmcnt(14)\n"
    "v_mfma_f32_16x16x32_bf16 v[130:133], v[46:49], v[190:193], v[130:133]\n"
    "ds_read_b64_tr_b16 v[72:73], v39 offset:2112\n"
    "v_mfma_f32_16x16x32_bf16 v[134:137], v[190:193], v[190:193], v[134:137]\n"
    "ds_read_b64_tr_b16 v[74:75], v39 offset:2624\n"
    "v_mfma_f32_16x16x32_bf16 v[126:129], v[24:27], v[142:145], v[126:129]\n"
    "v_mfma_f32_32x32x16_bf16 v[94:109], v[142:145], v[162:165], v[94:109]\n"
    "v_mfma_f32_32x32x16_bf16 v[110:125], v[142:145], v[166:169], v[110:125]\n"
    "s_waitcnt vmcnt(12) lgkmcnt(0)\n"
    "s_barrier\n"
    "ds_read_b64_tr_b16 v[138:139], v79\n"
    "ds_read_b64_tr_b16 v[140:141], v79 offset:512\n"
    "v_mfma_f32_16x16x32_bf16 v[126:129], v[24:27], v[146:149], v[126:129]\n"
    "buffer_store_dwordx4 v[90:93], v14, s[16:19], s54 offen sc1\n"
    "ds_read_b128 v[154:157], v81 offset:32768\n"
    "v_mfma_f32_32x32x16_bf16 v[94:109], v[146:149], v[170:173], v[94:109]\n"
    "ds_read_b128 v[158:161], v81 offset:36864\n"
    "buffer_store_dwordx4 v[90:93], v7, s[16:19], s54 offen offset:1024 sc1\n"
    "ds_read_b64_tr_b16 v[142:143], v79 offset:4096\n"
    "ds_read_b64_tr_b16 v[144:145], v79 offset:4608\n"
    "ds_read_b128 v[162:165], v82 offset:32768\n"
    "buffer_store_dwordx4 v[90:93], v41, s[16:19], s87 offen sc1\n"
    "v_mfma_f32_32x32x16_bf16 v[110:125], v[146:149], v[174:177], v[110:125]\n"
    "ds_read_b128 v[166:169], v82 offset:36864\n"
    "ds_read_b128 v[186:189], v87 offset:40960\n"
    "buffer_store_dwordx4 v[60:63], v40, s[16:19], s89 offen sc1\n"
    "ds_read_b128 v[190:193], v88 offset:40960\n"
    "v_mfma_f32_16x16x32_bf16 v[126:129], v[24:27], v[150:153], v[126:129]\n"
    "ds_read_b64_tr_b16 v[146:147], v79 offset:8192\n"
    "buffer_store_dwordx4 v[64:67], v40, s[16:19], s89 offen offset:1024 sc1\n"
    "v_mfma_f32_32x32x16_bf16 v[94:109], v[150:153], v[178:181], v[94:109]\n"
    "ds_read_b64_tr_b16 v[148:149], v79 offset:8704\n"
    "ds_read_b128 v[170:173], v83 offset:32768\n"
    "buffer_store_dwordx4 v[68:71], v17, s[16:19], s90 offen sc1\n"
    "ds_read_b128 v[174:177], v83 offset:36864\n"
    "v_mfma_f32_32x32x16_bf16 v[110:125], v[150:153], v[182:185], v[110:125]\n"
    "s_waitcnt lgkmcnt(12)\n"
    "v_mfma_f32_16x16x32_bf16 v[126:129], v[24:27], v[138:141], v[126:129]\n"
    "buffer_store_dwordx4 v[72:75], v43, s[16:19], s90 offen sc1\n"
    "ds_read_b64_tr_b16 v[150:151], v79 offset:12288\n"
    "s_waitcnt lgkmcnt(12)\n"
    "v_mfma_f32_32x32x16_bf16 v[94:109], v[138:141], v[154:157], v[94:109]\n"
    "ds_read_b64_tr_b16 v[152:153], v79 offset:12800\n"
    "s_add_u32 m0, s66, 0x1c000\n"
    "s_and_b32 s64, s59, 0x380\n"
    "s_mul_i32 s64, s64, 0x600\n"
    "s_add_u32 s64, s64, s67\n"
    "buffer_load_dwordx4 v52, s[12:15], s64 offen lds\n"
    "ds_read_b128 v[178:181], v84 offset:32768\n"
    "ds_read_b128 v[182:185], v84 offset:36864\n"
    "buffer_load_dwordx4 v53, s[12:15], s64 offen offset:1024 lds\n"
    "v_xor_b32 v238, v45, v238\n"
    "s_waitcnt lgkmcnt(14)\n"
    "v_mfma_f32_32x32x16_bf16 v[110:125], v[138:141], v[158:161], v[110:125]\n"
    "buffer_load_dwordx4 v54, s[12:15], s64 offen offset:2048 lds\n"
    "v_xor_b32 v239, v45, v239\n"
    "v_xor_b32 v240, v45, v240\n"
    "v_xor_b32 v241, v45, v241\n"
    "s_waitcnt lgkmcnt(9)\n"
    "v_mfma_f32_16x16x32_bf16 v[130:133], v[46:49], v[186:189], v[130:133]\n"
    "buffer_load_dwordx4 v55, s[12:15], s64 offen offset:3072 lds\n"
    "v_mfma_f32_16x16x32_bf16 v[134:137], v[186:189], v[186:189], v[134:137]\n"
    "s_waitcnt lgkmcnt(8)\n"
    "v_mfma_f32_16x16x32_bf16 v[130:133], v[46:49], v[190:193], v[130:133]\n"
    "v_mfma_f32_16x16x32_bf16 v[134:137], v[190:193], v[190:193], v[134:137]\n"
    "v_mfma_f32_16x16x32_bf16 v[126:129], v[24:27], v[142:145], v[126:129]\n"
    "v_mfma_f32_32x32x16_bf16 v[94:109], v[142:145], v[162:165], v[94:109]\n"
    "v_mfma_f32_32x32x16_bf16 v[110:125], v[142:145], v[166:169], v[110:125]\n"
    "s_waitcnt vmcnt(17) lgkmcnt(0)\n"
    "s_barrier\n"
    "ds_read_b64_tr_b16 v[138:139], v79 offset:16384\n"
    "ds_read_b64_tr_b16 v[140:141], v79 offset:16896\n"
    "v_mfma_f32_16x16x32_bf16 v[126:129], v[24:27], v[146:149], v[126:129]\n"
    "ds_read_b128 v[154:157], v81 offset:40960\n"
    "v_mfma_f32_32x32x16_bf16 v[94:109], v[146:149], v[170:173], v[94:109]\n"
    "ds_read_b128 v[158:161], v81 offset:45056\n"
    "ds_read_b64_tr_b16 v[142:143], v79 offset:20480\n"
    "ds_read_b64_tr_b16 v[144:145], v79 offset:20992\n"
    "ds_read_b128 v[162:165], v82 offset:40960\n"
    "v_mfma_f32_32x32x16_bf16 v[110:125], v[146:149], v[174:177], v[110:125]\n"
    "ds_read_b128 v[166:169], v82 offset:45056\n"
    "ds_read_b128 v[186:189], v87 offset:49152\n"
    "ds_read_b128 v[190:193], v88 offset:49152\n"
    "v_mfma_f32_16x16x32_bf16 v[126:129], v[24:27], v[150:153], v[126:129]\n"
    "ds_read_b64_tr_b16 v[146:147], v79 offset:24576\n"
    "v_mfma_f32_32x32x16_bf16 v[94:109], v[150:153], v[178:181], v[94:109]\n"
    "ds_read_b64_tr_b16 v[148:149], v79 offset:25088\n"
    "ds_read_b128 v[170:173], v83 offset:40960\n"
    "ds_read_b128 v[174:177], v83 offset:45056\n"
    "v_mfma_f32_32x32x16_bf16 v[110:125], v[150:153], v[182:185], v[110:125]\n"
    "s_waitcnt lgkmcnt(12)\n"
    "v_mfma_f32_16x16x32_bf16 v[126:129], v[24:27], v[138:141], v[126:129]\n"
    "ds_read_b64_tr_b16 v[150:151], v79 offset:28672\n"
    "s_waitcnt lgkmcnt(12)\n"
    "v_mfma_f32_32x32x16_bf16 v[94:109], v[138:141], v[154:157], v[94:109]\n"
    "ds_read_b64_tr_b16 v[152:153], v79 offset:29184\n"
    "ds_read_b128 v[178:181], v84 offset:40960\n"
    "ds_read_b128 v[182:185], v84 offset:45056\n"
    "s_waitcnt lgkmcnt(14)\n"
    "v_mfma_f32_32x32x16_bf16 v[110:125], v[138:141], v[158:161], v[110:125]\n"
    "s_waitcnt lgkmcnt(9)\n"
    "v_mfma_f32_16x16x32_bf16 v[130:133], v[46:49], v[186:189], v[130:133]\n"
    "v_mfma_f32_16x16x32_bf16 v[134:137], v[186:189], v[186:189], v[134:137]\n"
    "s_waitcnt lgkmcnt(8)\n"
    "v_mfma_f32_16x16x32_bf16 v[130:133], v[46:49], v[190:193], v[130:133]\n"
    "v_mfma_f32_16x16x32_bf16 v[134:137], v[190:193], v[190:193], v[134:137]\n"
    "v_mfma_f32_16x16x32_bf16 v[126:129], v[24:27], v[142:145], v[126:129]\n"
    "v_mfma_f32_32x32x16_bf16 v[94:109], v[142:145], v[162:165], v[94:109]\n"
    "v_mfma_f32_32x32x16_bf16 v[110:125], v[142:145], v[166:169], v[110:125]\n"
    "s_waitcnt vmcnt(11) lgkmcnt(0)\n"
    "s_barrier\n"
    "ds_read_b64_tr_b16 v[138:139], v79 offset:32768\n"
    "ds_read_b64_tr_b16 v[140:141], v79 offset:33280\n"
    "v_mfma_f32_16x16x32_bf16 v[126:129], v[24:27], v[146:149], v[126:129]\n"
    "ds_read_b128 v[154:157], v81 offset:49152\n"
    "v_mfma_f32_32x32x16_bf16 v[94:109], v[146:149], v[170:173], v[94:109]\n"
    "ds_read_b128 v[158:161], v81 offset:53248\n"
    "ds_read_b64_tr_b16 v[142:143], v79 offset:36864\n"
    "ds_read_b64_tr_b16 v[144:145], v79 offset:37376\n"
    "s_waitcnt vmcnt(4)\n"
    "buffer_store_dwordx2 v[12:13], v18, s[16:19], s86 offen sc1\n"
    "ds_read_b128 v[162:165], v82 offset:49152\n"
    "v_mfma_f32_32x32x16_bf16 v[110:125], v[146:149], v[174:177], v[110:125]\n"
    "ds_read_b128 v[166:169], v82 offset:53248\n"
    "ds_read_b128 v[186:189], v87 offset:57344\n"
    "ds_read_b128 v[190:193], v88 offset:57344\n"
    "v_mfma_f32_16x16x32_bf16 v[126:129], v[24:27], v[150:153], v[126:129]\n"
    "ds_read_b64_tr_b16 v[146:147], v79 offset:40960\n"
    "v_mfma_f32_32x32x16_bf16 v[94:109], v[150:153], v[178:181], v[94:109]\n"
    "s_mov_b32 s86, 0xaa000\n"
    "s_add_u32 m0, s66, 0x10000\n"
    "s_lshr_b32 s59, s49, 1\n"
    "s_add_u32 s59, s59, s68\n"
    "s_mul_i32 s67, s88, 0x18000\n"
    "s_lshl_b32 s64, s51, 7\n"
    "s_add_u32 s67, s67, s64\n"
    "s_and_b32 s64, s59, 7\n"
    "s_mul_i32 s64, s64, 0x30000\n"
    "s_add_u32 s64, s64, s67\n"
    "buffer_load_dwordx4 v56, s[12:15], s64 offen lds\n"
    "ds_read_b64_tr_b16 v[148:149], v79 offset:41472\n"
    "ds_read_b128 v[170:173], v83 offset:49152\n"
    "ds_read_b128 v[174:177], v83 offset:53248\n"
    "v_mfma_f32_32x32x16_bf16 v[110:125], v[150:153], v[182:185], v[110:125]\n"
    "s_waitcnt lgkmcnt(5)\n"
    "v_mfma_f32_16x16x32_bf16 v[130:133], v[46:49], v[186:189], v[130:133]\n"
    "ds_read_b64_tr_b16 v[150:151], v79 offset:45056\n"
    "v_mfma_f32_16x16x32_bf16 v[134:137], v[186:189], v[186:189], v[134:137]\n"
    "ds_read_b64_tr_b16 v[152:153], v79 offset:45568\n"
    "buffer_load_dwordx4 v57, s[12:15], s64 offen offset:1024 lds\n"
    "s_waitcnt lgkmcnt(6)\n"
    "v_mfma_f32_16x16x32_bf16 v[130:133], v[46:49], v[190:193], v[130:133]\n"
    "ds_read_b128 v[178:181], v84 offset:49152\n"
    "v_mfma_f32_16x16x32_bf16 v[134:137], v[190:193], v[190:193], v[134:137]\n"
    "ds_read_b128 v[182:185], v84 offset:53248\n"
    "v_mfma_f32_16x16x32_bf16 v[126:129], v[24:27], v[138:141], v[126:129]\n"
    "s_nop 3\n"
    "v_mul_f32 v2, 0xbb000000, v130\n"
    "v_mfma_f32_32x32x16_bf16 v[94:109], v[138:141], v[154:157], v[94:109]\n"
    "buffer_load_dwordx4 v58, s[12:15], s64 offen offset:2048 lds\n"
    "v_mul_f32 v3, v246, v134\n"
    "v_cvt_pk_bf16_f32 v250, v2, v2\n"
    "v_fmac_f32 v3, v247, v135\n"
    "v_mfma_f32_32x32x16_bf16 v[110:125], v[138:141], v[158:161], v[110:125]\n"
    "v_and_b32 v4, 0xffff0000, v250\n"
    "v_fmac_f32 v3, v248, v136\n"
    "v_sub_f32 v4, v2, v4\n"
    "v_fmac_f32 v3, v249, v137\n"
    "v_mfma_f32_16x16x32_bf16 v[126:129], v[24:27], v[142:145], v[126:129]\n"
    "buffer_load_dwordx4 v59, s[12:15], s64 offen offset:3072 lds\n"
    "v_cvt_pk_bf16_f32 v250, v2, v4\n"
    "v_mfma_f32_32x32x16_bf16 v[94:109], v[142:145], v[162:165], v[94:109]\n"
    "v_fma_f32 v3, -v2, v2, v3\n"
    "v_max_f32 v3, 0, v3\n"
    "v_add_f32 v3, 0x3727c5ac, v3\n"
    "v_mfma_f32_32x32x16_bf16 v[110:125], v[142:145], v[166:169], v[110:125]\n"
    "v_rsq_f32 v251, v3\n"
    "ds_write_b64 v34, v[250:251]\n"
    "s_waitcnt vmcnt(5) lgkmcnt(0)\n"
    "s_barrier\n"
    "ds_read_b64_tr_b16 v[138:139], v79 offset:49152\n"
    "ds_read_b64_tr_b16 v[140:141], v79 offset:49664\n"
    "v_mfma_f32_16x16x32_bf16 v[126:129], v[24:27], v[146:149], v[126:129]\n"
    "ds_read_b128 v[154:157], v81 offset:57344\n"
    "v_mfma_f32_32x32x16_bf16 v[94:109], v[146:149], v[170:173], v[94:109]\n"
    "ds_read_b128 v[158:161], v81 offset:61440\n"
    "s_add_u32 m0, s66, 0x14000\n"
    "s_add_u32 s59, s59, 2\n"
    "s_and_b32 s64, s59, 7\n"
    "s_mul_i32 s64, s64, 0x30000\n"
    "s_add_u32 s64, s64, s67\n"
    "buffer_load_dwordx4 v56, s[12:15], s64 offen lds\n"
    "ds_read_b64_tr_b16 v[142:143], v79 offset:53248\n"
    "ds_read_b64_tr_b16 v[144:145], v79 offset:53760\n"
    "ds_read_b128 v[162:165], v82 offset:57344\n"
    "v_mfma_f32_32x32x16_bf16 v[110:125], v[146:149], v[174:177], v[110:125]\n"
    "ds_read_b128 v[166:169], v82 offset:61440\n"
    "ds_read_b64 v[234:235], v35\n"
    "buffer_load_dwordx4 v57, s[12:15], s64 offen offset:1024 lds\n"
    "ds_read_b64 v[236:237], v35 offset:256\n"
    "ds_read_b32 v214, v36\n"
    "v_mfma_f32_16x16x32_bf16 v[126:129], v[24:27], v[150:153], v[126:129]\n"
    "ds_read_b32 v218, v36 offset:128\n"
    "v_mfma_f32_32x32x16_bf16 v[94:109], v[150:153], v[178:181], v[94:109]\n"
    "ds_read_b32 v222, v36 offset:256\n"
    "buffer_load_dwordx4 v58, s[12:15], s64 offen offset:2048 lds\n"
    "ds_read_b32 v226, v36 offset:384\n"
    "ds_read_b64_tr_b16 v[146:147], v79 offset:57344\n"
    "ds_read_b64_tr_b16 v[148:149], v79 offset:57856\n"
    "v_mfma_f32_32x32x16_bf16 v[110:125], v[150:153], v[182:185], v[110:125]\n"
    "ds_read_b128 v[170:173], v83 offset:57344\n"
    "ds_read_b128 v[174:177], v83 offset:61440\n"
    "buffer_load_dwordx4 v59, s[12:15], s64 offen offset:3072 lds\n"
    "s_waitcnt lgkmcnt(15)\n"
    "v_mfma_f32_16x16x32_bf16 v[126:129], v[24:27], v[138:141], v[126:129]\n"
    "ds_read_b64_tr_b16 v[150:151], v79 offset:61440\n"
    "ds_read_b64_tr_b16 v[152:153], v79 offset:61952\n"
    "v_mfma_f32_32x32x16_bf16 v[94:109], v[138:141], v[154:157], v[94:109]\n"
    "ds_read_b128 v[178:181], v84 offset:57344\n"
    "ds_read_b128 v[182:185], v84 offset:61440\n"
    "v_cmp_gt_u32 vcc, 32, v1\n"
    "s_waitcnt lgkmcnt(13)\n"
    "v_mul_f32 v230, 0x3fb8aa3b, v235\n"
    "s_waitcnt lgkmcnt(12)\n"
    "v_mul_f32 v232, 0x3fb8aa3b, v237\n"
    "v_mfma_f32_16x16x32_bf16 v[126:129], v[24:27], v[142:145], v[126:129]\n"
    "v_mfma_f32_32x32x16_bf16 v[94:109], v[142:145], v[162:165], v[94:109]\n"
    "v_cndmask_b32 v206, 0, v234, vcc\n"
    "v_cndmask_b32 v207, 0, v234, vcc\n"
    "v_cndmask_b32 v210, 0, v236, vcc\n"
    "v_cndmask_b32 v211, 0, v236, vcc\n"
    "s_waitcnt lgkmcnt(6)\n"
    "v_mfma_f32_16x16x32_bf16 v[126:129], v[24:27], v[146:149], v[126:129]\n"
    "s_waitcnt lgkmcnt(2)\n"
    "s_barrier\n"
    "v_mfma_f32_16x16x32_bf16 v[126:129], v[24:27], v[150:153], v[126:129]\n"
    "v_mfma_f32_32x32x16_bf16 v[94:109], v[146:149], v[170:173], v[94:109]\n"
    "s_waitcnt lgkmcnt(1)\n"
    "v_mfma_f32_32x32x16_bf16 v[94:109], v[150:153], v[178:181], v[94:109]\n"
    "s_nop 4\n"
    "v_cvt_pk_bf16_f32 v202, v126, v126\n"
    "v_and_b32 v3, 0xffff0000, v202\n"
    "v_sub_f32 v3, v126, v3\n"
    "v_cvt_pk_bf16_f32 v203, v3, v3\n"
    "v_mfma_f32_32x32x16_bf16 v[110:125], v[138:141], v[158:161], v[110:125]\n"
    "s_nop 0\n"
    "v_mfma_f32_32x32x16_bf16 v[94:109], v[202:205], v[206:209], v[94:109]\n"
    "v_cmp_gt_u32 vcc, 16, v1\n"
    "v_mfma_f32_32x32x16_bf16 v[110:125], v[142:145], v[166:169], v[110:125]\n"
    "s_nop 0\n"
    "v_cndmask_b32 v214, 0, v214, vcc\n"
    "v_mov_b32 v215, v214\n"
    "v_cndmask_b32 v218, 0, v218, vcc\n"
    "v_mov_b32 v219, v218\n"
    "v_mfma_f32_32x32x16_bf16 v[110:125], v[146:149], v[174:177], v[110:125]\n"
    "v_cndmask_b32 v222, 0, v222, vcc\n"
    "v_mov_b32 v223, v222\n"
    "v_cndmask_b32 v226, 0, v226, vcc\n"
    "v_mov_b32 v227, v226\n"
    "v_cvt_pk_bf16_f32 v186, v94, v95\n"
    "v_cvt_pk_bf16_f32 v187, v96, v97\n"
    "v_cvt_pk_bf16_f32 v188, v98, v99\n"
    "v_cvt_pk_bf16_f32 v189, v100, v101\n"
    "s_waitcnt lgkmcnt(0)\n"
    "v_mfma_f32_32x32x16_bf16 v[110:125], v[150:153], v[182:185], v[110:125]\n"
    "v_mul_f32 v102, v235, v102\n"
    "v_mul_f32 v103, v235, v103\n"
    "v_mul_f32 v104, v235, v104\n"
    "v_mul_f32 v105, v235, v105\n"
    "v_mul_f32 v106, v235, v106\n"
    "v_mfma_f32_32x32x16_bf16 v[30:45], v[238:241], v[186:189], 0\n"
    "v_mul_f32 v107, v235, v107\n"
    "v_mul_f32 v108, v235, v108\n"
    "v_mul_f32 v109, v235, v109\n"
    "v_cvt_pk_bf16_f32 v190, v102, v103\n"
    "v_cvt_pk_bf16_f32 v191, v104, v105\n"
    "v_cvt_pk_bf16_f32 v192, v106, v107\n"
    "v_mfma_f32_32x32x16_bf16 v[110:125], v[202:205], v[210:213], v[110:125]\n"
    "v_cvt_pk_bf16_f32 v193, v108, v109\n"
    "ds_write_b128 v242, v[190:193]\n"
    "ds_read_b64_tr_b16 v[130:131], v244\n"
    "ds_read_b64_tr_b16 v[132:133], v244 offset:192\n"
    "s_nop 0\n"
    "v_max3_f32 v60, v30, v31, v32\n"
    "v_max3_f32 v61, v33, v34, v35\n"
    "v_max3_f32 v62, v36, v37, v38\n"
    "v_max3_f32 v63, v39, v40, v41\n"
    "v_max3_f32 v64, v42, v43, v44\n"
    "v_max3_f32 v60, v60, v61, v45\n"
    "v_max3_f32 v62, v62, v63, v64\n"
    "v_max_f32 v60, v60, v62\n"
    "v_mul_f32 v231, v230, v60\n"
    "v_cvt_pk_bf16_f32 v138, v110, v111\n"
    "v_cvt_pk_bf16_f32 v139, v112, v113\n"
    "v_cvt_pk_bf16_f32 v140, v114, v115\n"
    "v_cvt_pk_bf16_f32 v141, v116, v117\n"
    "v_mul_f32 v118, v237, v118\n"
    "v_mul_f32 v119, v237, v119\n"
    "v_mfma_f32_32x32x16_bf16 v[170:185], v[238:241], v[138:141], 0\n"
    "v_mul_f32 v120, v237, v120\n"
    "v_mul_f32 v121, v237, v121\n"
    "v_mul_f32 v122, v237, v122\n"
    "v_mul_f32 v123, v237, v123\n"
    "v_mul_f32 v124, v237, v124\n"
    "v_mul_f32 v125, v237, v125\n"
    "v_cvt_pk_bf16_f32 v146, v118, v119\n"
    "v_cvt_pk_bf16_f32 v147, v120, v121\n"
    "v_cvt_pk_bf16_f32 v148, v122, v123\n"
    "v_cvt_pk_bf16_f32 v149, v124, v125\n"
    "ds_write_b128 v242, v[146:149]\n"
    "ds_read_b64_tr_b16 v[134:135], v244\n"
    "ds_read_b64_tr_b16 v[136:137], v244 offset:192\n"
    "s_add_u32 m0, s66, 0x18000\n"
    "s_add_u32 s59, s59, 2\n"
    "s_and_b32 s64, s59, 7\n"
    "s_mul_i32 s64, s64, 0x30000\n"
    "s_add_u32 s64, s64, s67\n"
    "buffer_load_dwordx4 v56, s[12:15], s64 offen lds\n"
    "buffer_load_dwordx4 v57, s[12:15], s64 offen offset:1024 lds\n"
    "buffer_load_dwordx4 v58, s[12:15], s64 offen offset:2048 lds\n"
    "buffer_load_dwordx4 v59, s[12:15], s64 offen offset:3072 lds\n"
    "v_pk_fma_f32 v[30:31], v[30:31], v[230:231], v[230:231] op_sel:[0,0,1] op_sel_hi:[1,0,1] neg_lo:[0,0,1] neg_hi:[0,0,1]\n"
    "v_pk_fma_f32 v[32:33], v[32:33], v[230:231], v[230:231] op_sel:[0,0,1] op_sel_hi:[1,0,1] neg_lo:[0,0,1] neg_hi:[0,0,1]\n"
    "v_pk_fma_f32 v[34:35], v[34:35], v[230:231], v[230:231] op_sel:[0,0,1] op_sel_hi:[1,0,1] neg_lo:[0,0,1] neg_hi:[0,0,1]\n"
    "v_pk_fma_f32 v[36:37], v[36:37], v[230:231], v[230:231] op_sel:[0,0,1] op_sel_hi:[1,0,1] neg_lo:[0,0,1] neg_hi:[0,0,1]\n"
    "v_pk_fma_f32 v[38:39], v[38:39], v[230:231], v[230:231] op_sel:[0,0,1] op_sel_hi:[1,0,1] neg_lo:[0,0,1] neg_hi:[0,0,1]\n"
    "v_pk_fma_f32 v[40:41], v[40:41], v[230:231], v[230:231] op_sel:[0,0,1] op_sel_hi:[1,0,1] neg_lo:[0,0,1] neg_hi:[0,0,1]\n"
    "v_pk_fma_f32 v[42:43], v[42:43], v[230:231], v[230:231] op_sel:[0,0,1] op_sel_hi:[1,0,1] neg_lo:[0,0,1] neg_hi:[0,0,1]\n"
    "v_pk_fma_f32 v[44:45], v[44:45], v[230:231], v[230:231] op_sel:[0,0,1] op_sel_hi:[1,0,1] neg_lo:[0,0,1] neg_hi:[0,0,1]\n"
    "v_exp_f32 v30, v30\n"
    "v_exp_f32 v31, v31\n"
    "v_exp_f32 v32, v32\n"
    "v_exp_f32 v33, v33\n"
    "v_exp_f32 v34, v34\n"
    "v_exp_f32 v35, v35\n"
    "v_exp_f32 v36, v36\n"
    "v_exp_f32 v37, v37\n"
    "v_exp_f32 v38, v38\n"
    "v_exp_f32 v39, v39\n"
    "v_exp_f32 v40, v40\n"
    "v_exp_f32 v41, v41\n"
    "v_exp_f32 v42, v42\n"
    "v_exp_f32 v43, v43\n"
    "v_exp_f32 v44, v44\n"
    "v_exp_f32 v45, v45\n"
    "v_pk_add_f32 v[60:61], v[30:31], v[32:33]\n"
    "v_pk_add_f32 v[62:63], v[34:35], v[36:37]\n"
    "v_pk_add_f32 v[64:65], v[38:39], v[40:41]\n"
    "v_pk_add_f32 v[66:67], v[42:43], v[44:45]\n"
    "v_pk_add_f32 v[60:61], v[60:61], v[62:63]\n"
    "v_max3_f32 v68, v170, v171, v172\n"
    "v_pk_add_f32 v[64:65], v[64:65], v[66:67]\n"
    "v_max3_f32 v69, v173, v174, v175\n"
    "v_pk_add_f32 v[60:61], v[60:61], v[64:65]\n"
    "v_max3_f32 v70, v176, v177, v178\n"
    "v_add_f32 v60, v60, v61\n"
    "v_max3_f32 v71, v179, v180, v181\n"
    "v_rcp_f32 v231, v60\n"
    "v_max3_f32 v72, v182, v183, v184\n"
    "v_max3_f32 v68, v68, v69, v185\n"
    "v_max3_f32 v70, v70, v71, v72\n"
    "v_max_f32 v68, v68, v70\n"
    "v_mul_f32 v233, v232, v68\n"
    "v_pk_mul_f32 v[30:31], v[30:31], v[230:231] op_sel:[0,1] op_sel_hi:[1,1]\n"
    "v_pk_mul_f32 v[32:33], v[32:33], v[230:231] op_sel:[0,1] op_sel_hi:[1,1]\n"
    "v_pk_mul_f32 v[34:35], v[34:35], v[230:231] op_sel:[0,1] op_sel_hi:[1,1]\n"
    "v_pk_mul_f32 v[36:37], v[36:37], v[230:231] op_sel:[0,1] op_sel_hi:[1,1]\n"
    "v_pk_mul_f32 v[38:39], v[38:39], v[230:231] op_sel:[0,1] op_sel_hi:[1,1]\n"
    "v_pk_mul_f32 v[40:41], v[40:41], v[230:231] op_sel:[0,1] op_sel_hi:[1,1]\n"
    "v_pk_mul_f32 v[42:43], v[42:43], v[230:231] op_sel:[0,1] op_sel_hi:[1,1]\n"
    "v_pk_mul_f32 v[44:45], v[44:45], v[230:231] op_sel:[0,1] op_sel_hi:[1,1]\n"
    "v_cvt_pk_bf16_f32 v60, v30, v31\n"
    "v_cvt_pk_bf16_f32 v61, v32, v33\n"
    "v_cvt_pk_bf16_f32 v62, v34, v35\n"
    "v_cvt_pk_bf16_f32 v63, v36, v37\n"
    "v_cvt_pk_bf16_f32 v64, v38, v39\n"
    "v_cvt_pk_bf16_f32 v65, v40, v41\n"
    "v_cvt_pk_bf16_f32 v66, v42, v43\n"
    "v_cvt_pk_bf16_f32 v67, v44, v45\n"
    "ds_write_b128 v89, v[60:63]\n"
    "ds_write_b128 v89, v[64:67] offset:16\n"
    "ds_read_b64_tr_b16 v[154:155], v243\n"
    "ds_read_b64_tr_b16 v[156:157], v243 offset:320\n"
    "ds_read_b64_tr_b16 v[158:159], v243 offset:8\n"
    "ds_read_b64_tr_b16 v[160:161], v243 offset:328\n"
    "s_add_u32 m0, s66, 0x1c000\n"
    "s_add_u32 s59, s59, 2\n"
    "s_and_b32 s64, s59, 7\n"
    "s_mul_i32 s64, s64, 0x30000\n"
    "s_add_u32 s64, s64, s67\n"
    "buffer_load_dwordx4 v56, s[12:15], s64 offen lds\n"
    "buffer_load_dwordx4 v57, s[12:15], s64 offen offset:1024 lds\n"
    "buffer_load_dwordx4 v58, s[12:15], s64 offen offset:2048 lds\n"
    "buffer_load_dwordx4 v59, s[12:15], s64 offen offset:3072 lds\n"
    "buffer_load_dwordx2 v[10:11], v19, s[16:19], s86 offen sc1\n"
    "v_pk_fma_f32 v[170:171], v[170:171], v[232:233], v[232:233] op_sel:[0,0,1] op_sel_hi:[1,0,1] neg_lo:[0,0,1] neg_hi:[0,0,1]\n"
    "v_pk_fma_f32 v[172:173], v[172:173], v[232:233], v[232:233] op_sel:[0,0,1] op_sel_hi:[1,0,1] neg_lo:[0,0,1] neg_hi:[0,0,1]\n"
    "v_pk_fma_f32 v[174:175], v[174:175], v[232:233], v[232:233] op_sel:[0,0,1] op_sel_hi:[1,0,1] neg_lo:[0,0,1] neg_hi:[0,0,1]\n"
    "v_pk_fma_f32 v[176:177], v[176:177], v[232:233], v[232:233] op_sel:[0,0,1] op_sel_hi:[1,0,1] neg_lo:[0,0,1] neg_hi:[0,0,1]\n"
    "v_pk_fma_f32 v[178:179], v[178:179], v[232:233], v[232:233] op_sel:[0,0,1] op_sel_hi:[1,0,1] neg_lo:[0,0,1] neg_hi:[0,0,1]\n"
    "v_pk_fma_f32 v[180:181], v[180:181], v[232:233], v[232:233] op_sel:[0,0,1] op_sel_hi:[1,0,1] neg_lo:[0,0,1] neg_hi:[0,0,1]\n"
    "v_pk_fma_f32 v[182:183], v[182:183], v[232:233], v[232:233] op_sel:[0,0,1] op_sel_hi:[1,0,1] neg_lo:[0,0,1] neg_hi:[0,0,1]\n"
    "v_pk_fma_f32 v[184:185], v[184:185], v[232:233], v[232:233] op_sel:[0,0,1] op_sel_hi:[1,0,1] neg_lo:[0,0,1] neg_hi:[0,0,1]\n"
    "v_exp_f32 v170, v170\n"
    "v_exp_f32 v171, v171\n"
    "v_exp_f32 v172, v172\n"
    "v_exp_f32 v173, v173\n"
    "v_exp_f32 v174, v174\n"
    "v_exp_f32 v175, v175\n"
    "v_exp_f32 v176, v176\n"
    "v_exp_f32 v177, v177\n"
    "v_exp_f32 v178, v178\n"
    "v_exp_f32 v179, v179\n"
    "s_waitcnt lgkmcnt(2)\n"
    "v_mfma_f32_16x16x32_bf16 v[186:189], v[154:157], v[130:133], 0\n"
    "v_exp_f32 v180, v180\n"
    "s_waitcnt lgkmcnt(0)\n"
    "v_mfma_f32_16x16x32_bf16 v[190:193], v[158:161], v[130:133], 0\n"
    "v_exp_f32 v181, v181\n"
    "v_mfma_f32_16x16x32_bf16 v[194:197], v[154:157], v[46:49], 0\n"
    "v_exp_f32 v182, v182\n"
    "v_mfma_f32_16x16x32_bf16 v[198:201], v[158:161], v[46:49], 0\n"
    "v_exp_f32 v183, v183\n"
    "v_exp_f32 v184, v184\n"
    "v_exp_f32 v185, v185\n"
    "v_pk_add_f32 v[68:69], v[170:171], v[172:173]\n"
    "v_pk_add_f32 v[70:71], v[174:175], v[176:177]\n"
    "v_pk_add_f32 v[72:73], v[178:179], v[180:181]\n"
    "v_pk_add_f32 v[74:75], v[182:183], v[184:185]\n"
    "v_pk_add_f32 v[68:69], v[68:69], v[70:71]\n"
    "v_pk_add_f32 v[72:73], v[72:73], v[74:75]\n"
    "v_pk_add_f32 v[68:69], v[68:69], v[72:73]\n"
    "v_add_f32 v68, v68, v69\n"
    "v_rcp_f32 v233, v68\n"
    "s_nop 0\n"
    "v_pk_mul_f32 v[170:171], v[170:171], v[232:233] op_sel:[0,1] op_sel_hi:[1,1]\n"
    "v_pk_mul_f32 v[172:173], v[172:173], v[232:233] op_sel:[0,1] op_sel_hi:[1,1]\n"
    "v_pk_mul_f32 v[174:175], v[174:175], v[232:233] op_sel:[0,1] op_sel_hi:[1,1]\n"
    "v_pk_mul_f32 v[176:177], v[176:177], v[232:233] op_sel:[0,1] op_sel_hi:[1,1]\n"
    "v_pk_mul_f32 v[178:179], v[178:179], v[232:233] op_sel:[0,1] op_sel_hi:[1,1]\n"
    "v_pk_mul_f32 v[180:181], v[180:181], v[232:233] op_sel:[0,1] op_sel_hi:[1,1]\n"
    "v_pk_mul_f32 v[182:183], v[182:183], v[232:233] op_sel:[0,1] op_sel_hi:[1,1]\n"
    "v_pk_mul_f32 v[184:185], v[184:185], v[232:233] op_sel:[0,1] op_sel_hi:[1,1]\n"
    "v_cvt_pk_bf16_f32 v68, v170, v171\n"
    "v_cvt_pk_bf16_f32 v69, v172, v173\n"
    "v_cvt_pk_bf16_f32 v70, v174, v175\n"
    "v_cvt_pk_bf16_f32 v71, v176, v177\n"
    "v_cvt_pk_bf16_f32 v72, v178, v179\n"
    "v_cvt_pk_bf16_f32 v73, v180, v181\n"
    "v_cvt_pk_bf16_f32 v74, v182, v183\n"
    "v_cvt_pk_bf16_f32 v75, v184, v185\n"
    "ds_write_b128 v89, v[68:71]\n"
    "ds_write_b128 v89, v[72:75] offset:16\n"
    "ds_read_b64_tr_b16 v[154:155], v243\n"
    "ds_read_b64_tr_b16 v[156:157], v243 offset:320\n"
    "ds_read_b64_tr_b16 v[158:159], v243 offset:8\n"
    "ds_read_b64_tr_b16 v[160:161], v243 offset:328\n"
    "s_waitcnt lgkmcnt(2)\n"
    "v_mfma_f32_16x16x32_bf16 v[186:189], v[154:157], v[134:137], v[186:189]\n"
    "s_waitcnt lgkmcnt(0)\n"
    "v_mfma_f32_16x16x32_bf16 v[190:193], v[158:161], v[134:137], v[190:193]\n"
    "v_mfma_f32_16x16x32_bf16 v[194:197], v[154:157], v[46:49], v[194:197]\n"
    "v_mfma_f32_16x16x32_bf16 v[198:201], v[158:161], v[46:49], v[198:201]\n"
    "s_nop 3\n"
    "v_cvt_pk_bf16_f32 v60, v186, v187\n"
    "v_cvt_pk_bf16_f32 v61, v188, v189\n"
    "v_cvt_pk_bf16_f32 v62, v190, v191\n"
    "v_cvt_pk_bf16_f32 v63, v192, v193\n"
    "v_cvt_pk_bf16_f32 v64, v194, v195\n"
    "v_cvt_pk_bf16_f32 v65, v196, v197\n"
    "v_cvt_pk_bf16_f32 v66, v198, v199\n"
    "v_cvt_pk_bf16_f32 v67, v200, v201\n"
    "buffer_store_dwordx4 v[60:63], v14, s[16:19], s54 offen sc1\n"
    "buffer_store_dwordx4 v[64:67], v7, s[16:19], s54 offen offset:1024 sc1\n"
    "v_min_u32 v42, 4, v1\n"
    "v_mul_u32_u24 v42, 0x20000, v42\n"
    "s_waitcnt vmcnt(11)\n"
    "s_barrier\n"
    "ds_read_b64_tr_b16 v[138:139], v80\n"
    "ds_read_b64_tr_b16 v[140:141], v80 offset:512\n"
    "ds_read_b128 v[154:157], v85\n"
    "ds_read_b128 v[158:161], v85 offset:2048\n"
    "ds_read_b128 v[162:165], v85 offset:4096\n"
    "ds_read_b128 v[166:169], v85 offset:6144\n"
    "ds_read_b64_tr_b16 v[142:143], v80 offset:4096\n"
    "ds_read_b64_tr_b16 v[144:145], v80 offset:4608\n"
    "ds_read_b128 v[170:173], v86\n"
    "ds_read_b128 v[174:177], v86 offset:2048\n"
    "ds_read_b128 v[178:181], v86 offset:4096\n"
    "ds_read_b128 v[182:185], v86 offset:6144\n"
    "s_waitcnt lgkmcnt(10)\n"
    "v_mfma_f32_16x16x32_bf16 v[126:129], v[46:49], v[138:141], 0\n"
    "ds_read_b64_tr_b16 v[146:147], v80 offset:8192\n"
    "ds_read_b64_tr_b16 v[148:149], v80 offset:8704\n"
    "s_waitcnt lgkmcnt(11)\n"
    "v_mfma_f32_16x16x32_bf16 v[94:97], v[138:141], v[154:157], 0\n"
    "ds_read_b128 v[186:189], v85 offset:8192\n"
    "s_waitcnt lgkmcnt(11)\n"
    "v_mfma_f32_16x16x32_bf16 v[98:101], v[138:141], v[158:161], 0\n"
    "ds_read_b128 v[190:193], v85 offset:10240\n"
    "s_waitcnt lgkmcnt(11)\n"
    "v_mfma_f32_16x16x32_bf16 v[102:105], v[138:141], v[162:165], 0\n"
    "ds_read_b128 v[194:197], v85 offset:12288\n"
    "s_waitcnt lgkmcnt(11)\n"
    "v_mfma_f32_16x16x32_bf16 v[106:109], v[138:141], v[166:169], 0\n"
    "s_waitcnt lgkmcnt(5)\n"
    "ds_read_b128 v[198:201], v85 offset:14336\n"
    "s_waitcnt vmcnt(2)\n"
    "v_cmp_ne_u64 vcc, s[56:57], v[10:11]\n"
    "s_nop 0\n"
    "s_cbranch_vccnz .Lhh_p1\n"
    ".Lhh_p1d:\n"
    "v_mfma_f32_16x16x32_bf16 v[126:129], v[46:49], v[142:145], v[126:129]\n"
    "ds_read_b64_tr_b16 v[138:139], v80 offset:12288\n"
    "ds_read_b64_tr_b16 v[140:141], v80 offset:12800\n"
    "v_mfma_f32_16x16x32_bf16 v[94:97], v[142:145], v[170:173], v[94:97]\n"
    "ds_read_b128 v[154:157], v86 offset:8192\n"
    "v_mfma_f32_16x16x32_bf16 v[98:101], v[142:145], v[174:177], v[98:101]\n"
    "ds_read_b128 v[158:161], v86 offset:10240\n"
    "v_mfma_f32_16x16x32_bf16 v[102:105], v[142:145], v[178:181], v[102:105]\n"
    "ds_read_b128 v[162:165], v86 offset:12288\n"
    "v_mfma_f32_16x16x32_bf16 v[106:109], v[142:145], v[182:185], v[106:109]\n"
    "s_waitcnt lgkmcnt(5)\n"
    "ds_read_b128 v[166:169], v86 offset:14336\n"
    "s_barrier\n"
    "v_mfma_f32_16x16x32_bf16 v[126:129], v[46:49], v[146:149], v[126:129]\n"
    "ds_read_b64_tr_b16 v[142:143], v80 offset:16384\n"
    "ds_read_b64_tr_b16 v[144:145], v80 offset:16896\n"
    "s_lshl_b32 s91, s76, 14\n"
    "s_add_u32 s91, s91, 0xca000\n"
    "buffer_load_dwordx4 a[192:195], v14, s[16:19], s91 offen sc1\n"
    "v_mfma_f32_16x16x32_bf16 v[94:97], v[146:149], v[186:189], v[94:97]\n"
    "ds_read_b128 v[170:173], v85 offset:16384\n"
    "v_mfma_f32_16x16x32_bf16 v[98:101], v[146:149], v[190:193], v[98:101]\n"
    "ds_read_b128 v[174:177], v85 offset:18432\n"
    "v_mfma_f32_16x16x32_bf16 v[102:105], v[146:149], v[194:197], v[102:105]\n"
    "ds_read_b128 v[178:181], v85 offset:20480\n"
    "v_mfma_f32_16x16x32_bf16 v[106:109], v[146:149], v[198:201], v[106:109]\n"
    "s_waitcnt lgkmcnt(5)\n"
    "ds_read_b128 v[182:185], v85 offset:22528\n"
    "v_mfma_f32_16x16x32_bf16 v[126:129], v[46:49], v[138:141], v[126:129]\n"
    "ds_read_b64_tr_b16 v[146:147], v80 offset:20480\n"
    "ds_read_b64_tr_b16 v[148:149], v80 offset:20992\n"
    "buffer_load_dwordx4 a[196:199], v14, s[16:19], s91 offen offset:1024 sc1\n"
    "v_mfma_f32_16x16x32_bf16 v[94:97], v[138:141], v[154:157], v[94:97]\n"
    "ds_read_b128 v[186:189], v86 offset:16384\n"
    "v_mfma_f32_16x16x32_bf16 v[98:101], v[138:141], v[158:161], v[98:101]\n"
    "ds_read_b128 v[190:193], v86 offset:18432\n"
    "v_mfma_f32_16x16x32_bf16 v[102:105], v[138:141], v[162:165], v[102:105]\n"
    "ds_read_b128 v[194:197], v86 offset:20480\n"
    "v_mfma_f32_16x16x32_bf16 v[106:109], v[138:141], v[166:169], v[106:109]\n"
    "s_waitcnt lgkmcnt(5)\n"
    "ds_read_b128 v[198:201], v86 offset:22528\n"
    "v_mfma_f32_16x16x32_bf16 v[126:129], v[46:49], v[142:145], v[126:129]\n"
    "ds_read_b64_tr_b16 v[138:139], v80 offset:24576\n"
    "ds_read_b64_tr_b16 v[140:141], v80 offset:25088\n"
    "buffer_load_dwordx4 a[200:203], v14, s[16:19], s91 offen offset:2048 sc1\n"
    "v_mfma_f32_16x16x32_bf16 v[94:97], v[142:145], v[170:173], v[94:97]\n"
    "ds_read_b128 v[154:157], v85 offset:24576\n"
    "v_mfma_f32_16x16x32_bf16 v[98:101], v[142:145], v[174:177], v[98:101]\n"
    "ds_read_b128 v[158:161], v85 offset:26624\n"
    "v_mfma_f32_16x16x32_bf16 v[102:105], v[142:145], v[178:181], v[102:105]\n"
    "ds_read_b128 v[162:165], v85 offset:28672\n"
    "v_mfma_f32_16x16x32_bf16 v[106:109], v[142:145], v[182:185], v[106:109]\n"
    "s_waitcnt lgkmcnt(5)\n"
    "ds_read_b128 v[166:169], v85 offset:30720\n"
    "v_mfma_f32_16x16x32_bf16 v[126:129], v[46:49], v[146:149], v[126:129]\n"
    "ds_read_b64_tr_b16 v[142:143], v80 offset:28672\n"
    "ds_read_b64_tr_b16 v[144:145], v80 offset:29184\n"
    "buffer_load_dwordx4 a[204:207], v14, s[16:19], s91 offen offset:3072 sc1\n"
    "v_mfma_f32_16x16x32_bf16 v[94:97], v[146:149], v[186:189], v[94:97]\n"
    "ds_read_b128 v[170:173], v86 offset:24576\n"
    "v_mfma_f32_16x16x32_bf16 v[98:101], v[146:149], v[190:193], v[98:101]\n"
    "ds_read_b128 v[174:177], v86 offset:26624\n"
    "v_mfma_f32_16x16x32_bf16 v[102:105], v[146:149], v[194:197], v[102:105]\n"
    "ds_read_b128 v[178:181], v86 offset:28672\n"
    "v_mfma_f32_16x16x32_bf16 v[106:109], v[146:149], v[198:201], v[106:109]\n"
    "s_waitcnt lgkmcnt(5)\n"
    "ds_read_b128 v[182:185], v86 offset:30720\n"
    "v_mfma_f32_16x16x32_bf16 v[126:129], v[46:49], v[138:141], v[126:129]\n"
    "ds_read_b64_tr_b16 v[146:147], v80 offset:32768\n"
    "ds_read_b64_tr_b16 v[148:149], v80 offset:33280\n"
    "s_add_u32 s91, s91, 0x1000\n"
    "buffer_load_dwordx4 a[208:211], v14, s[16:19], s91 offen sc1\n"
    "v_mfma_f32_16x16x32_bf16 v[94:97], v[138:141], v[154:157], v[94:97]\n"
    "ds_read_b128 v[186:189], v85 offset:32768\n"
    "v_mfma_f32_16x16x32_bf16 v[98:101], v[138:141], v[158:161], v[98:101]\n"
    "ds_read_b128 v[190:193], v85 offset:34816\n"
    "v_mfma_f32_16x16x32_bf16 v[102:105], v[138:141], v[162:165], v[102:105]\n"
    "ds_read_b128 v[194:197], v85 offset:36864\n"
    "v_mfma_f32_16x16x32_bf16 v[106:109], v[138:141], v[166:169], v[106:109]\n"
    "s_waitcnt lgkmcnt(5)\n"
    "ds_read_b128 v[198:201], v85 offset:38912\n"
    "buffer_load_dwordx4 v[130:133], v5, s[16:19], s55 offen sc1\n"
    "buffer_load_dwordx4 v[134:137], v6, s[16:19], s55 offen offset:1024 sc1\n"
    "v_mfma_f32_16x16x32_bf16 v[126:129], v[46:49], v[142:145], v[126:129]\n"
    "ds_read_b64_tr_b16 v[138:139], v80 offset:36864\n"
    "ds_read_b64_tr_b16 v[140:141], v80 offset:37376\n"
    "buffer_load_dwordx4 a[212:215], v14, s[16:19], s91 offen offset:1024 sc1\n"
    "v_mfma_f32_16x16x32_bf16 v[94:97], v[142:145], v[170:173], v[94:97]\n"
    "ds_read_b128 v[154:157], v86 offset:32768\n"
    "v_mfma_f32_16x16x32_bf16 v[98:101], v[142:145], v[174:177], v[98:101]\n"
    "ds_read_b128 v[158:161], v86 offset:34816\n"
    "buffer_load_dwordx4 a[216:219], v14, s[16:19], s91 offen offset:2048 sc1\n"
    "v_mfma_f32_16x16x32_bf16 v[102:105], v[142:145], v[178:181], v[102:105]\n"
    "ds_read_b128 v[162:165], v86 offset:36864\n"
    "v_mfma_f32_16x16x32_bf16 v[106:109], v[142:145], v[182:185], v[106:109]\n"
    "s_waitcnt lgkmcnt(5)\n"
    "ds_read_b128 v[166:169], v86 offset:38912\n"
    "v_mfma_f32_16x16x32_bf16 v[126:129], v[46:49], v[146:149], v[126:129]\n"
    "ds_read_b64_tr_b16 v[142:143], v80 offset:40960\n"
    "ds_read_b64_tr_b16 v[144:145], v80 offset:41472\n"
    "buffer_load_dwordx4 a[220:223], v14, s[16:19], s91 offen offset:3072 sc1\n"
    "v_mfma_f32_16x16x32_bf16 v[94:97], v[146:149], v[186:189], v[94:97]\n"
    "ds_read_b128 v[170:173], v85 offset:40960\n"
    "v_mfma_f32_16x16x32_bf16 v[98:101], v[146:149], v[190:193], v[98:101]\n"
    "ds_read_b128 v[174:177], v85 offset:43008\n"
    "s_add_u32 s91, s91, 0x1000\n"
    "buffer_load_dwordx4 a[224:227], v14, s[16:19], s91 offen sc1\n"
    "v_mfma_f32_16x16x32_bf16 v[102:105], v[146:149], v[194:197], v[102:105]\n"
    "ds_read_b128 v[178:181], v85 offset:45056\n"
    "v_mfma_f32_16x16x32_bf16 v[106:109], v[146:149], v[198:201], v[106:109]\n"
    "s_waitcnt lgkmcnt(5)\n"
    "ds_read_b128 v[182:185], v85 offset:47104\n"
    "v_mfma_f32_16x16x32_bf16 v[126:129], v[46:49], v[138:141], v[126:129]\n"
    "ds_read_b64_tr_b16 v[146:147], v80 offset:45056\n"
    "ds_read_b64_tr_b16 v[148:149], v80 offset:45568\n"
    "buffer_load_dwordx4 a[228:231], v14, s[16:19], s91 offen offset:1024 sc1\n"
    "v_mfma_f32_16x16x32_bf16 v[94:97], v[138:141], v[154:157], v[94:97]\n"
    "ds_read_b128 v[186:189], v86 offset:40960\n"
    "v_mfma_f32_16x16x32_bf16 v[98:101], v[138:141], v[158:161], v[98:101]\n"
    "ds_read_b128 v[190:193], v86 offset:43008\n"
    "v_mfma_f32_16x16x32_bf16 v[102:105], v[138:141], v[162:165], v[102:105]\n"
    "ds_read_b128 v[194:197], v86 offset:45056\n"
    "buffer_load_dwordx4 a[232:235], v14, s[16:19], s91 offen offset:2048 sc1\n"
    "v_mfma_f32_16x16x32_bf16 v[106:109], v[138:141], v[166:169], v[106:109]\n"
    "s_waitcnt lgkmcnt(5)\n"
    "ds_read_b128 v[198:201], v86 offset:47104\n"
    "v_mfma_f32_16x16x32_bf16 v[126:129], v[46:49], v[142:145], v[126:129]\n"
    "ds_read_b64_tr_b16 v[138:139], v80 offset:49152\n"
    "ds_read_b64_tr_b16 v[140:141], v80 offset:49664\n"
    "buffer_load_dwordx4 a[236:239], v14, s[16:19], s91 offen offset:3072 sc1\n"
    "v_mfma_f32_16x16x32_bf16 v[94:97], v[142:145], v[170:173], v[94:97]\n"
    "ds_read_b128 v[154:157], v85 offset:49152\n"
    "v_mfma_f32_16x16x32_bf16 v[98:101], v[142:145], v[174:177], v[98:101]\n"
    "ds_read_b128 v[158:161], v85 offset:51200\n"
    "v_mfma_f32_16x16x32_bf16 v[102:105], v[142:145], v[178:181], v[102:105]\n"
    "ds_read_b128 v[162:165], v85 offset:53248\n"
    "s_add_u32 s91, s91, 0x1000\n"
    "buffer_load_dwordx4 a[240:243], v14, s[16:19], s91 offen sc1\n"
    "v_mfma_f32_16x16x32_bf16 v[106:109], v[142:145], v[182:185], v[106:109]\n"
    "s_waitcnt lgkmcnt(5)\n"
    "ds_read_b128 v[166:169], v85 offset:55296\n"
    "v_mfma_f32_16x16x32_bf16 v[126:129], v[46:49], v[146:149], v[126:129]\n"
    "ds_read_b64_tr_b16 v[142:143], v80 offset:53248\n"
    "ds_read_b64_tr_b16 v[144:145], v80 offset:53760\n"
    "v_mfma_f32_16x16x32_bf16 v[94:97], v[146:149], v[186:189], v[94:97]\n"
    "ds_read_b128 v[170:173], v86 offset:49152\n"
    "v_mfma_f32_16x16x32_bf16 v[98:101], v[146:149], v[190:193], v[98:101]\n"
    "ds_read_b128 v[174:177], v86 offset:51200\n"
    "v_mfma_f32_16x16x32_bf16 v[102:105], v[146:149], v[194:197], v[102:105]\n"
    "ds_read_b128 v[178:181], v86 offset:53248\n"
    "v_mfma_f32_16x16x32_bf16 v[106:109], v[146:149], v[198:201], v[106:109]\n"
    "s_waitcnt lgkmcnt(5)\n"
    "ds_read_b128 v[182:185], v86 offset:55296\n"
    "v_mfma_f32_16x16x32_bf16 v[126:129], v[46:49], v[138:141], v[126:129]\n"
    "ds_read_b64_tr_b16 v[146:147], v80 offset:57344\n"
    "ds_read_b64_tr_b16 v[148:149], v80 offset:57856\n"
    "v_mfma_f32_16x16x32_bf16 v[94:97], v[138:141], v[154:157], v[94:97]\n"
    "ds_read_b128 v[186:189], v85 offset:57344\n"
    "v_mfma_f32_16x16x32_bf16 v[98:101], v[138:141], v[158:161], v[98:101]\n"
    "ds_read_b128 v[190:193], v85 offset:59392\n"
    "v_mfma_f32_16x16x32_bf16 v[102:105], v[138:141], v[162:165], v[102:105]\n"
    "ds_read_b128 v[194:197], v85 offset:61440\n"
    "v_mfma_f32_16x16x32_bf16 v[106:109], v[138:141], v[166:169], v[106:109]\n"
    "s_waitcnt lgkmcnt(5)\n"
    "ds_read_b128 v[198:201], v85 offset:63488\n"
    "s_waitcnt vmcnt(9)\n"
    "v_max3_u32 v10, v130, v131, v132\n"
    "s_waitcnt vmcnt(8)\n"
    "v_max3_u32 v11, v133, v134, v135\n"
    "v_max3_u32 v10, v10, v136, v137\n"
    "v_max_u32 v10, v10, v11\n"
    "v_cmp_eq_u32 vcc, -1, v10\n"
    "s_nop 0\n"
    "s_cbranch_vccnz .Lhh_v2\n"
    ".Lhh_v2d:\n"
    "v_mfma_f32_32x32x16_bf16 v[60:75], v[130:133], a[184:187], 0\n"
    "v_mfma_f32_32x32x16_bf16 v[60:75], v[134:137], a[188:191], v[60:75]\n"
    "v_mfma_f32_16x16x32_bf16 v[126:129], v[46:49], v[142:145], v[126:129]\n"
    "ds_read_b64_tr_b16 v[138:139], v80 offset:61440\n"
    "ds_read_b64_tr_b16 v[140:141], v80 offset:61952\n"
    "v_mfma_f32_16x16x32_bf16 v[94:97], v[142:145], v[170:173], v[94:97]\n"
    "ds_read_b128 v[154:157], v86 offset:57344\n"
    "v_mfma_f32_16x16x32_bf16 v[98:101], v[142:145], v[174:177], v[98:101]\n"
    "ds_read_b128 v[158:161], v86 offset:59392\n"
    "v_mfma_f32_16x16x32_bf16 v[102:105], v[142:145], v[178:181], v[102:105]\n"
    "ds_read_b128 v[162:165], v86 offset:61440\n"
    "v_mfma_f32_16x16x32_bf16 v[106:109], v[142:145], v[182:185], v[106:109]\n"
    "s_waitcnt lgkmcnt(5)\n"
    "ds_read_b128 v[166:169], v86 offset:63488\n"
    "v_add_f32 v60, v60, v68\n"
    "v_add_f32 v61, v61, v69\n"
    "v_add_f32 v62, v62, v70\n"
    "v_add_f32 v63, v63, v71\n"
    "v_add_f32 v64, v64, v72\n"
    "v_add_f32 v65, v65, v73\n"
    "v_add_f32 v66, v66, v74\n"
    "v_add_f32 v67, v67, v75\n"
    "v_add_f32 v60, v60, v64\n"
    "v_add_f32 v61, v61, v65\n"
    "v_add_f32 v62, v62, v66\n"
    "v_add_f32 v63, v63, v67\n"
    "v_add_f32 v60, v60, v62\n"
    "v_add_f32 v61, v61, v63\n"
    "v_add_f32 v60, v60, v61\n"
    "v_mov_b32 v61, v60\n"
    "s_mul_i32 s54, s76, 0x480\n"
    "s_add_u32 s54, s54, 0x1000\n"
    "v_permlane32_swap_b32 v60, v61\n"
    "v_add_f32 v60, v60, v61\n"
    "v_and_b32 v6, 48, v1\n"
    "s_nop 0\n"
    "v_mov_b32 v61, v60 quad_perm:[1,0,3,2] row_mask:0xf bank_mask:0xf\n"
    "v_cvt_pk_bf16_f32 v60, v60, v61\n"
    "buffer_store_dword v60, v245, s[16:19], s87 offen sc1\n"
    "buffer_load_dword a180, v42, s[16:19], s78 offen sc1\n"
    "v_mfma_f32_16x16x32_bf16 v[126:129], v[46:49], v[146:149], v[126:129]\n"
    "v_mfma_f32_16x16x32_bf16 v[94:97], v[146:149], v[186:189], v[94:97]\n"
    "v_mfma_f32_16x16x32_bf16 v[98:101], v[146:149], v[190:193], v[98:101]\n"
    "v_mfma_f32_16x16x32_bf16 v[102:105], v[146:149], v[194:197], v[102:105]\n"
    "v_mfma_f32_16x16x32_bf16 v[106:109], v[146:149], v[198:201], v[106:109]\n"
    "s_waitcnt lgkmcnt(0)\n"
    "v_mfma_f32_16x16x32_bf16 v[126:129], v[46:49], v[138:141], v[126:129]\n"
    "v_mfma_f32_16x16x32_bf16 v[94:97], v[138:141], v[154:157], v[94:97]\n"
    "v_mfma_f32_16x16x32_bf16 v[98:101], v[138:141], v[158:161], v[98:101]\n"
    "v_mfma_f32_16x16x32_bf16 v[102:105], v[138:141], v[162:165], v[102:105]\n"
    "v_mfma_f32_16x16x32_bf16 v[106:109], v[138:141], v[166:169], v[106:109]\n"
    "buffer_load_dwordx4 a[244:247], v14, s[16:19], s91 offen offset:1024 sc1\n"
    "buffer_load_dwordx4 a[248:251], v14, s[16:19], s91 offen offset:2048 sc1\n"
    "s_nop 1\n"
    "v_cvt_pk_bf16_f32 v202, v126, v126\n"
    "v_and_b32 v3, 0xffff0000, v202\n"
    "v_sub_f32 v3, v126, v3\n"
    "v_cvt_pk_bf16_f32 v203, v3, v3\n"
    "s_nop 1\n"
    "v_mfma_f32_16x16x32_bf16 v[94:97], v[202:205], v[214:217], v[94:97]\n"
    "v_mfma_f32_16x16x32_bf16 v[98:101], v[202:205], v[218:221], v[98:101]\n"
    "v_mfma_f32_16x16x32_bf16 v[102:105], v[202:205], v[222:225], v[102:105]\n"
    "v_mfma_f32_16x16x32_bf16 v[106:109], v[202:205], v[226:229], v[106:109]\n"
    "buffer_load_dwordx4 a[252:255], v14, s[16:19], s91 offen offset:3072 sc1\n"
    "s_lshl1_add_u32 s91, s79, s88\n"
    "s_lshl_b32 s91, s91, 14\n"
    "s_add_u32 s91, s91, 0x2aa000\n"
    "buffer_load_dwordx4 a[0:3], v14, s[16:19], s91 offen sc1\n"
    "v_cvt_pk_bf16_f32 v138, v94, v95\n"
    "v_cvt_pk_bf16_f32 v139, v96, v97\n"
    "v_cvt_pk_bf16_f32 v140, v98, v99\n"
    "v_cvt_pk_bf16_f32 v141, v100, v101\n"
    "v_cvt_pk_bf16_f32 v142, v102, v103\n"
    "v_cvt_pk_bf16_f32 v143, v104, v105\n"
    "v_cvt_pk_bf16_f32 v144, v106, v107\n"
    "v_cvt_pk_bf16_f32 v145, v108, v109\n"
    "v_permlane16_swap_b32 v138, v140\n"
    "v_permlane16_swap_b32 v139, v141\n"
    "v_permlane16_swap_b32 v142, v144\n"
    "v_permlane16_swap_b32 v143, v145\n"
    "v_mfma_f32_32x32x16_bf16 v[170:185], v[238:241], v[138:141], 0\n"
    "s_nop 0\n"
    "v_mfma_f32_32x32x16_bf16 v[110:125], v[238:241], v[142:145], 0\n"
    "buffer_load_dwordx4 a[4:7], v14, s[16:19], s91 offen offset:1024 sc1\n"
    "buffer_load_dwordx4 a[8:11], v14, s[16:19], s91 offen offset:2048 sc1\n"
    "s_nop 7\n"
    "v_max3_f32 v60, v170, v171, v172\n"
    "v_max3_f32 v61, v173, v174, v175\n"
    "v_max3_f32 v62, v176, v177, v178\n"
    "v_max3_f32 v63, v179, v180, v181\n"
    "v_max3_f32 v64, v182, v183, v184\n"
    "v_max3_f32 v60, v60, v61, v185\n"
    "v_max3_f32 v68, v110, v111, v112\n"
    "v_max3_f32 v62, v62, v63, v64\n"
    "v_max3_f32 v69, v113, v114, v115\n"
    "v_max_f32 v60, v60, v62\n"
    "v_max3_f32 v70, v116, v117, v118\n"
    "v_mul_f32 v231, v230, v60\n"
    "v_max3_f32 v71, v119, v120, v121\n"
    "v_pk_fma_f32 v[170:171], v[170:171], v[230:231], v[230:231] op_sel:[0,0,1] op_sel_hi:[1,0,1] neg_lo:[0,0,1] neg_hi:[0,0,1]\n"
    "buffer_load_dwordx4 a[12:15], v14, s[16:19], s91 offen offset:3072 sc1\n"
    "s_add_u32 s91, s91, 0x1000\n"
    "buffer_load_dwordx4 a[16:19], v14, s[16:19], s91 offen sc1\n"
    "v_max3_f32 v72, v122, v123, v124\n"
    "v_pk_fma_f32 v[172:173], v[172:173], v[230:231], v[230:231] op_sel:[0,0,1] op_sel_hi:[1,0,1] neg_lo:[0,0,1] neg_hi:[0,0,1]\n"
    "v_max3_f32 v68, v68, v69, v125\n"
    "v_pk_fma_f32 v[174:175], v[174:175], v[230:231], v[230:231] op_sel:[0,0,1] op_sel_hi:[1,0,1] neg_lo:[0,0,1] neg_hi:[0,0,1]\n"
    "v_max3_f32 v70, v70, v71, v72\n"
    "v_pk_fma_f32 v[176:177], v[176:177], v[230:231], v[230:231] op_sel:[0,0,1] op_sel_hi:[1,0,1] neg_lo:[0,0,1] neg_hi:[0,0,1]\n"
    "v_max_f32 v68, v68, v70\n"
    "v_pk_fma_f32 v[178:179], v[178:179], v[230:231], v[230:231] op_sel:[0,0,1] op_sel_hi:[1,0,1] neg_lo:[0,0,1] neg_hi:[0,0,1]\n"
    "v_mul_f32 v233, v232, v68\n"
    "v_pk_fma_f32 v[180:181], v[180:181], v[230:231], v[230:231] op_sel:[0,0,1] op_sel_hi:[1,0,1] neg_lo:[0,0,1] neg_hi:[0,0,1]\n"
    "v_pk_fma_f32 v[110:111], v[110:111], v[232:233], v[232:233] op_sel:[0,0,1] op_sel_hi:[1,0,1] neg_lo:[0,0,1] neg_hi:[0,0,1]\n"
    "v_pk_fma_f32 v[182:183], v[182:183], v[230:231], v[230:231] op_sel:[0,0,1] op_sel_hi:[1,0,1] neg_lo:[0,0,1] neg_hi:[0,0,1]\n"
    "buffer_load_dwordx4 a[20:23], v14, s[16:19], s91 offen offset:1024 sc1\n"
    "buffer_load_dwordx4 a[24:27], v14, s[16:19], s91 offen offset:2048 sc1\n"
    "v_pk_fma_f32 v[112:113], v[112:113], v[232:233], v[232:233] op_sel:[0,0,1] op_sel_hi:[1,0,1] neg_lo:[0,0,1] neg_hi:[0,0,1]\n"
    "v_pk_fma_f32 v[184:185], v[184:185], v[230:231], v[230:231] op_sel:[0,0,1] op_sel_hi:[1,0,1] neg_lo:[0,0,1] neg_hi:[0,0,1]\n"
    "v_pk_fma_f32 v[114:115], v[114:115], v[232:233], v[232:233] op_sel:[0,0,1] op_sel_hi:[1,0,1] neg_lo:[0,0,1] neg_hi:[0,0,1]\n"
    "v_exp_f32 v170, v170\n"
    "v_pk_fma_f32 v[116:117], v[116:117], v[232:233], v[232:233] op_sel:[0,0,1] op_sel_hi:[1,0,1] neg_lo:[0,0,1] neg_hi:[0,0,1]\n"
    "v_exp_f32 v171, v171\n"
    "v_pk_fma_f32 v[118:119], v[118:119], v[232:233], v[232:233] op_sel:[0,0,1] op_sel_hi:[1,0,1] neg_lo:[0,0,1] neg_hi:[0,0,1]\n"
    "v_exp_f32 v172, v172\n"
    "v_pk_fma_f32 v[120:121], v[120:121], v[232:233], v[232:233] op_sel:[0,0,1] op_sel_hi:[1,0,1] neg_lo:[0,0,1] neg_hi:[0,0,1]\n"
    "v_exp_f32 v173, v173\n"
    "v_pk_fma_f32 v[122:123], v[122:123], v[232:233], v[232:233] op_sel:[0,0,1] op_sel_hi:[1,0,1] neg_lo:[0,0,1] neg_hi:[0,0,1]\n"
    "v_exp_f32 v174, v174\n"
    "buffer_load_dwordx4 a[28:31], v14, s[16:19], s91 offen offset:3072 sc1\n"
    "s_add_u32 s91, s91, 0x1000\n"
    "buffer_load_dwordx4 a[32:35], v14, s[16:19], s91 offen sc1\n"
    "v_pk_fma_f32 v[124:125], v[124:125], v[232:233], v[232:233] op_sel:[0,0,1] op_sel_hi:[1,0,1] neg_lo:[0,0,1] neg_hi:[0,0,1]\n"
    "v_exp_f32 v175, v175\n"
    "v_exp_f32 v110, v110\n"
    "v_exp_f32 v176, v176\n"
    "v_exp_f32 v111, v111\n"
    "v_exp_f32 v177, v177\n"
    "v_exp_f32 v112, v112\n"
    "v_exp_f32 v178, v178\n"
    "v_exp_f32 v113, v113\n"
    "v_exp_f32 v179, v179\n"
    "v_exp_f32 v114, v114\n"
    "v_exp_f32 v180, v180\n"
    "buffer_load_dwordx4 a[36:39], v14, s[16:19], s91 offen offset:1024 sc1\n"
    "buffer_load_dwordx4 a[40:43], v14, s[16:19], s91 offen offset:2048 sc1\n"
    "v_exp_f32 v115, v115\n"
    "v_exp_f32 v181, v181\n"
    "v_exp_f32 v116, v116\n"
    "v_exp_f32 v182, v182\n"
    "v_exp_f32 v117, v117\n"
    "v_exp_f32 v183, v183\n"
    "v_exp_f32 v118, v118\n"
    "v_exp_f32 v184, v184\n"
    "v_exp_f32 v119, v119\n"
    "v_exp_f32 v185, v185\n"
    "v_exp_f32 v120, v120\n"
    "buffer_load_dwordx4 a[44:47], v14, s[16:19], s91 offen offset:3072 sc1\n"
    "s_add_u32 s91, s91, 0x1000\n"
    "buffer_load_dwordx4 a[48:51], v14, s[16:19], s91 offen sc1\n"
    "v_pk_add_f32 v[60:61], v[170:171], v[172:173]\n"
    "v_exp_f32 v121, v121\n"
    "v_pk_add_f32 v[62:63], v[174:175], v[176:177]\n"
    "v_exp_f32 v122, v122\n"
    "v_pk_add_f32 v[64:65], v[178:179], v[180:181]\n"
    "v_exp_f32 v123, v123\n"
    "v_pk_add_f32 v[66:67], v[182:183], v[184:185]\n"
    "v_exp_f32 v124, v124\n"
    "v_pk_add_f32 v[60:61], v[60:61], v[62:63]\n"
    "v_exp_f32 v125, v125\n"
    "v_pk_add_f32 v[64:65], v[64:65], v[66:67]\n"
    "v_pk_add_f32 v[68:69], v[110:111], v[112:113]\n"
    "v_pk_add_f32 v[60:61], v[60:61], v[64:65]\n"
    "v_pk_add_f32 v[70:71], v[114:115], v[116:117]\n"
    "v_add_f32 v60, v60, v61\n"
    "v_pk_add_f32 v[72:73], v[118:119], v[120:121]\n"
    "v_rcp_f32 v231, v60\n"
    "v_pk_add_f32 v[74:75], v[122:123], v[124:125]\n"
    "v_pk_mul_f32 v[170:171], v[170:171], v[230:231] op_sel:[0,1] op_sel_hi:[1,1]\n"
    "v_pk_add_f32 v[68:69], v[68:69], v[70:71]\n"
    "v_pk_mul_f32 v[172:173], v[172:173], v[230:231] op_sel:[0,1] op_sel_hi:[1,1]\n"
    "v_pk_add_f32 v[72:73], v[72:73], v[74:75]\n"
    "v_pk_mul_f32 v[174:175], v[174:175], v[230:231] op_sel:[0,1] op_sel_hi:[1,1]\n"
    "v_pk_add_f32 v[68:69], v[68:69], v[72:73]\n"
    "v_pk_mul_f32 v[176:177], v[176:177], v[230:231] op_sel:[0,1] op_sel_hi:[1,1]\n"
    "v_add_f32 v68, v68, v69\n"
    "v_pk_mul_f32 v[178:179], v[178:179], v[230:231] op_sel:[0,1] op_sel_hi:[1,1]\n"
    "v_rcp_f32 v233, v68\n"
    "v_pk_mul_f32 v[180:181], v[180:181], v[230:231] op_sel:[0,1] op_sel_hi:[1,1]\n"
    "v_pk_mul_f32 v[110:111], v[110:111], v[232:233] op_sel:[0,1] op_sel_hi:[1,1]\n"
    "v_pk_mul_f32 v[182:183], v[182:183], v[230:231] op_sel:[0,1] op_sel_hi:[1,1]\n"
    "v_pk_mul_f32 v[112:113], v[112:113], v[232:233] op_sel:[0,1] op_sel_hi:[1,1]\n"
    "v_pk_mul_f32 v[184:185], v[184:185], v[230:231] op_sel:[0,1] op_sel_hi:[1,1]\n"
    "v_pk_mul_f32 v[114:115], v[114:115], v[232:233] op_sel:[0,1] op_sel_hi:[1,1]\n"
    "v_cvt_pk_bf16_f32 v60, v170, v171\n"
    "v_pk_mul_f32 v[116:117], v[116:117], v[232:233] op_sel:[0,1] op_sel_hi:[1,1]\n"
    "v_cvt_pk_bf16_f32 v61, v172, v173\n"
    "v_pk_mul_f32 v[118:119], v[118:119], v[232:233] op_sel:[0,1] op_sel_hi:[1,1]\n"
    "v_cvt_pk_bf16_f32 v62, v174, v175\n"
    "v_pk_mul_f32 v[120:121], v[120:121], v[232:233] op_sel:[0,1] op_sel_hi:[1,1]\n"
    "v_cvt_pk_bf16_f32 v63, v176, v177\n"
    "v_pk_mul_f32 v[122:123], v[122:123], v[232:233] op_sel:[0,1] op_sel_hi:[1,1]\n"
    "v_cvt_pk_bf16_f32 v64, v178, v179\n"
    "v_pk_mul_f32 v[124:125], v[124:125], v[232:233] op_sel:[0,1] op_sel_hi:[1,1]\n"
    "v_cvt_pk_bf16_f32 v65, v180, v181\n"
    "v_cvt_pk_bf16_f32 v68, v110, v111\n"
    "v_cvt_pk_bf16_f32 v66, v182, v183\n"
    "v_cvt_pk_bf16_f32 v69, v112, v113\n"
    "v_cvt_pk_bf16_f32 v67, v184, v185\n"
    "v_cvt_pk_bf16_f32 v70, v114, v115\n"
    "v_cvt_pk_bf16_f32 v71, v116, v117\n"
    "v_cvt_pk_bf16_f32 v72, v118, v119\n"
    "v_cvt_pk_bf16_f32 v73, v120, v121\n"
    "v_cvt_pk_bf16_f32 v74, v122, v123\n"
    "v_cvt_pk_bf16_f32 v75, v124, v125\n"
    "v_permlane16_swap_b32 v60, v64\n"
    "v_permlane16_swap_b32 v61, v65\n"
    "v_permlane16_swap_b32 v62, v66\n"
    "v_permlane16_swap_b32 v63, v67\n"
    "v_permlane16_swap_b32 v68, v72\n"
    "v_permlane16_swap_b32 v69, v73\n"
    "v_permlane16_swap_b32 v70, v74\n"
    "v_permlane16_swap_b32 v71, v75\n"
    "buffer_load_dwordx4 v[130:133], v14, s[16:19], s54 offen sc1\n"
    "buffer_load_dwordx4 v[134:137], v6, s[16:19], s54 offen offset:1024 sc1\n"
    "buffer_load_dwordx4 a[52:55], v14, s[16:19], s91 offen offset:1024 sc1\n"
    "buffer_load_dwordx4 a[56:59], v14, s[16:19], s91 offen offset:2048 sc1\n"
    "buffer_load_dwordx4 a[60:63], v14, s[16:19], s91 offen offset:3072 sc1\n"
    "s_lshl_b32 s72, s88, 15\n"
    "s_sub_u32 s91, s91, s72\n"
    "s_add_u32 s91, s91, 0x1000\n"
    "buffer_load_dwordx4 a[64:67], v14, s[16:19], s91 offen sc1\n"
    "s_waitcnt vmcnt(5)\n"
    "v_max3_u32 v10, v130, v131, v132\n"
    "s_waitcnt vmcnt(4)\n"
    "v_max3_u32 v11, v133, v134, v135\n"
    "v_max3_u32 v10, v10, v136, v137\n"
    "v_max_u32 v10, v10, v11\n"
    "v_cmp_eq_u32 vcc, -1, v10\n"
    "s_nop 0\n"
    "s_cbranch_vccnz .Lhh_v3\n"
    ".Lhh_v3d:\n"
    "v_mfma_f32_16x16x32_bf16 v[170:173], v[134:137], v[60:63], 0\n"
    "v_mfma_f32_16x16x32_bf16 v[174:177], v[134:137], v[64:67], 0\n"
    "v_mfma_f32_16x16x32_bf16 v[178:181], v[134:137], v[68:71], 0\n"
    "v_mfma_f32_16x16x32_bf16 v[182:185], v[134:137], v[72:75], 0\n"
    "v_mfma_f32_16x16x32_bf16 v[94:97], v[130:133], v[60:63], 0\n"
    "v_mfma_f32_16x16x32_bf16 v[98:101], v[130:133], v[64:67], 0\n"
    "v_mfma_f32_16x16x32_bf16 v[102:105], v[130:133], v[68:71], 0\n"
    "v_mfma_f32_16x16x32_bf16 v[106:109], v[130:133], v[72:75], 0\n"
    "s_nop 0\n"
    "v_add_f32 v170, 0x358637bd, v170\n"
    "v_add_f32 v174, 0x358637bd, v174\n"
    "v_add_f32 v178, 0x358637bd, v178\n"
    "v_add_f32 v182, 0x358637bd, v182\n"
    "v_rcp_f32 v50, v170\n"
    "v_rcp_f32 v51, v174\n"
    "v_rcp_f32 v52, v178\n"
    "v_rcp_f32 v53, v182\n"
    "v_mul_f32 v94, v50, v94\n"
    "v_mul_f32 v95, v50, v95\n"
    "v_mul_f32 v96, v50, v96\n"
    "v_mul_f32 v97, v50, v97\n"
    "v_mul_f32 v98, v51, v98\n"
    "v_mul_f32 v99, v51, v99\n"
    "v_mul_f32 v100, v51, v100\n"
    "v_mul_f32 v101, v51, v101\n"
    "v_cvt_pk_bf16_f32 v80, v94, v95\n"
    "v_cvt_pk_bf16_f32 v81, v96, v97\n"
    "v_cvt_pk_bf16_f32 v82, v98, v99\n"
    "v_cvt_pk_bf16_f32 v83, v100, v101\n"
    "v_mul_f32 v102, v52, v102\n"
    "v_mul_f32 v103, v52, v103\n"
    "v_mul_f32 v104, v52, v104\n"
    "v_mul_f32 v105, v52, v105\n"
    "v_mul_f32 v106, v53, v106\n"
    "v_mul_f32 v107, v53, v107\n"
    "v_mul_f32 v108, v53, v108\n"
    "v_mul_f32 v109, v53, v109\n"
    "v_cvt_pk_bf16_f32 v84, v102, v103\n"
    "v_cvt_pk_bf16_f32 v85, v104, v105\n"
    "v_cvt_pk_bf16_f32 v86, v106, v107\n"
    "v_cvt_pk_bf16_f32 v87, v108, v109\n"
    "v_permlane16_swap_b32 v80, v82\n"
    "v_permlane16_swap_b32 v81, v83\n"
    "v_permlane16_swap_b32 v84, v86\n"
    "v_permlane16_swap_b32 v85, v87\n"
    "buffer_store_dwordx4 v[80:83], v14, s[16:19], s83 offen sc1\n"
    "buffer_store_dwordx4 v[84:87], v14, s[16:19], s83 offen offset:1024 sc1\n"
    "buffer_load_dwordx4 a[68:71], v14, s[16:19], s91 offen offset:1024 sc1\n"
    "buffer_load_dwordx4 a[72:75], v14, s[16:19], s91 offen offset:2048 sc1\n"
    "buffer_load_dwordx4 a[76:79], v14, s[16:19], s91 offen offset:3072 sc1\n"
    "s_add_u32 s91, s91, 0x1000\n"
    "buffer_load_dwordx4 a[80:83], v14, s[16:19], s91 offen sc1\n"
    "buffer_load_dwordx4 a[84:87], v14, s[16:19], s91 offen offset:1024 sc1\n"
    "buffer_load_dwordx4 a[88:91], v14, s[16:19], s91 offen offset:2048 sc1\n"
    "s_sub_u32 s52, s51, s68\n"
    "s_and_b32 s52, s52, 7\n"
    "s_lshl_b32 s52, s52, 13\n"
    "s_add_u32 s52, s52, s75\n"
    "s_lshl_b32 s53, s88, 2\n"
    "v_lshrrev_b32 v2, 1, v9\n"
    "v_or_b32 v2, s53, v2\n"
    "v_xor_b32 v2, v2, v8\n"
    "v_lshlrev_b32 v2, 4, v2\n"
    "v_lshl_or_b32 v2, v9, 7, v2\n"
    "v_add_u32 v2, s52, v2\n"
    "ds_read_b128 v[34:37], v2\n"
    "ds_read_b128 v[42:45], v2 offset:4096\n"
    "s_or_b32 s82, s48, s49\n"
    "s_min_u32 s82, s82, 1\n"
    "s_mul_i32 s82, s82, 0x7ff00000\n"
    "v_or_b32 v6, s82, v18\n"
    "s_min_u32 s82, s49, 1\n"
    "s_mul_i32 s82, s82, 0x7ff00000\n"
    "v_cmp_gt_u32 vcc, 8, v1\n"
    "v_lshl_or_b32 v18, v1, 9, s82\n"
    "s_nop 0\n"
    "v_cndmask_b32 v18, v20, v18, vcc\n"
    "s_waitcnt lgkmcnt(1)\n"
    "v_lshlrev_b32 v30, 16, v34\n"
    "v_and_b32 v31, 0xffff0000, v34\n"
    "v_lshlrev_b32 v32, 16, v35\n"
    "v_and_b32 v33, 0xffff0000, v35\n"
    "v_lshlrev_b32 v34, 16, v36\n"
    "v_and_b32 v35, 0xffff0000, v36\n"
    "v_lshlrev_b32 v36, 16, v37\n"
    "v_and_b32 v37, 0xffff0000, v37\n"
    "s_waitcnt lgkmcnt(0)\n"
    "v_lshlrev_b32 v38, 16, v42\n"
    "v_and_b32 v39, 0xffff0000, v42\n"
    "v_lshlrev_b32 v40, 16, v43\n"
    "v_and_b32 v41, 0xffff0000, v43\n"
    "v_lshlrev_b32 v42, 16, v44\n"
    "v_and_b32 v43, 0xffff0000, v44\n"
    "v_lshlrev_b32 v44, 16, v45\n"
    "v_and_b32 v45, 0xffff0000, v45\n"
    "s_mul_i32 s52, s49, 0x4800\n"
    "s_add_u32 s52, s52, 0x12000\n"
    "v_add_u32 v19, s52, v14\n"
    "s_lshl_b32 s52, s49, 7\n"
    "s_add_u32 s52, s52, 0x12000\n"
    "v_mul_u32_u24 v2, 0x480, v8\n"
    "v_lshl_add_u32 v2, v9, 4, v2\n"
    "v_add_u32 v28, s52, v2\n"
    "v_lshlrev_b32 v2, 4, v9\n"
    "v_lshl_or_b32 v50, v8, 9, v2\n"
    "v_cmp_gt_u32 vcc, 32, v1\n"
    "v_lshlrev_b32 v3, 3, v23\n"
    "v_lshl_or_b32 v3, v29, 2, v3\n"
    "v_cndmask_b32 v51, v20, v3, vcc\n"
    "s_lshl2_add_u32 s83, s50, s49\n"
    "s_lshl_b32 s83, s83, 14\n"
    "s_add_u32 s83, s83, 0x522000\n"
    "s_lshl4_add_u32 s52, s50, s51\n"
    "s_lshl_b32 s52, s52, 5\n"
    "s_add_u32 s52, s52, s49\n"
    "s_lshl_b32 s52, s52, 7\n"
    "s_add_u32 s52, s52, 0x722000\n"
    "s_add_u32 s53, s52, 0x8000\n"
    "s_lshl_b32 s54, s50, 6\n"
    "s_add_u32 s54, s54, s76\n"
    "s_lshl_b32 s54, s54, 7\n"
    "s_add_u32 s54, s54, 0x922000\n"
    "s_add_u32 s55, s54, 0x1000\n"
    "v_mov_b32 v52, 0\n"
    "v_mov_b32 v53, 0\n"
    "v_mov_b32 v54, 0\n"
    "v_mov_b32 v55, 0\n"
    "s_waitcnt vmcnt(6)\n"
    "s_barrier\n"
    "buffer_store_dwordx2 v[12:13], v18, s[16:19], s77 offen sc1\n"
    "s_add_u32 s77, s77, 0x20000\n"
    "buffer_load_dwordx4 a[92:95], v14, s[16:19], s91 offen offset:3072 sc1\n"
    "s_add_u32 s91, s91, 0x1000\n"
    "buffer_load_dwordx4 a[96:99], v14, s[16:19], s91 offen sc1\n"
    "buffer_load_dwordx4 a[100:103], v14, s[16:19], s91 offen offset:1024 sc1\n"
    "buffer_load_dwordx4 a[104:107], v14, s[16:19], s91 offen offset:2048 sc1\n"
    "buffer_load_dwordx4 a[108:111], v14, s[16:19], s91 offen offset:3072 sc1\n"
    "s_add_u32 s91, s91, 0x1000\n"
    "buffer_load_dwordx4 a[112:115], v14, s[16:19], s91 offen sc1\n"
    "s_not_b64 s[60:61], s[56:57]\n"
    "s_not_b64 s[62:63], s[56:57]\n"
    "s_buffer_load_dwordx4 s[60:63], s[16:19], s78 glc\n"
    "buffer_load_dwordx4 a[116:119], v14, s[16:19], s91 offen offset:1024 sc1\n"
    "buffer_load_dwordx4 a[120:123], v14, s[16:19], s91 offen offset:2048 sc1\n"
    "buffer_load_dwordx4 a[124:127], v14, s[16:19], s91 offen offset:3072 sc1\n"
    "s_lshl1_add_u32 s91, s80, s88\n"
    "s_lshl_b32 s91, s91, 14\n"
    "s_add_u32 s91, s91, 0x2aa000\n"
    "buffer_load_dwordx4 a[128:131], v22, s[16:19], s91 offen sc1\n"
    "s_xor_b64 s[92:93], s[60:61], s[56:57]\n"
    "s_xor_b64 vcc, s[62:63], s[56:57]\n"
    "s_or_b64 s[92:93], s[92:93], vcc\n"
    "s_cbranch_scc0 .Lhh_s4d\n"
    "s_mov_b32 s58, 48\n"
    ".Lhh_s4a:\n"
    "s_buffer_load_dwordx4 s[60:63], s[16:19], s78 glc\n"
    "s_sleep 1\n"
    "s_xor_b64 s[92:93], s[60:61], s[56:57]\n"
    "s_xor_b64 vcc, s[62:63], s[56:57]\n"
    "s_or_b64 s[92:93], s[92:93], vcc\n"
    "s_cbranch_scc0 .Lhh_s4d\n"
    "s_sleep 1\n"
    "s_xor_b64 s[92:93], s[60:61], s[56:57]\n"
    "s_xor_b64 vcc, s[62:63], s[56:57]\n"
    "s_or_b64 s[92:93], s[92:93], vcc\n"
    "s_cbranch_scc0 .Lhh_s4d\n"
    "s_sleep 1\n"
    "s_xor_b64 s[92:93], s[60:61], s[56:57]\n"
    "s_xor_b64 vcc, s[62:63], s[56:57]\n"
    "s_or_b64 s[92:93], s[92:93], vcc\n"
    "s_cbranch_scc0 .Lhh_s4d\n"
    "s_sub_u32 s58, s58, 1\n"
    "s_cbranch_scc0 .Lhh_s4a\n"
    "s_branch .Lhh_s4\n"
    ".Lhh_s4d:\n"
    "s_add_u32 s82, s83, 0\n"
    "buffer_load_dwordx4 v[64:67], v14, s[16:19], s82 offen sc1\n"
    "buffer_load_dwordx4 v[68:71], v14, s[16:19], s82 offen offset:2048 sc1\n"
    "s_add_u32 s82, s83, 0x1000\n"
    "buffer_load_dwordx4 v[72:75], v14, s[16:19], s82 offen sc1\n"
    "buffer_load_dwordx4 v[76:79], v14, s[16:19], s82 offen offset:2048 sc1\n"
    "s_add_u32 s82, s83, 0x2000\n"
    "buffer_load_dwordx4 v[80:83], v14, s[16:19], s82 offen sc1\n"
    "buffer_load_dwordx4 v[84:87], v14, s[16:19], s82 offen offset:2048 sc1\n"
    "s_add_u32 s82, s83, 0x3000\n"
    "buffer_load_dwordx4 v[88:91], v14, s[16:19], s82 offen sc1\n"
    "buffer_load_dwordx4 v[92:95], v14, s[16:19], s82 offen offset:2048 sc1\n"
    "s_add_u32 s82, s83, 0\n"
    "buffer_load_dwordx4 v[96:99], v14, s[16:19], s82 offen offset:1024 sc1\n"
    "buffer_load_dwordx4 v[100:103], v14, s[16:19], s82 offen offset:3072 sc1\n"
    "s_add_u32 s82, s83, 0x1000\n"
    "buffer_load_dwordx4 v[104:107], v14, s[16:19], s82 offen offset:1024 sc1\n"
    "buffer_load_dwordx4 v[108:111], v14, s[16:19], s82 offen offset:3072 sc1\n"
    "s_add_u32 s82, s83, 0x2000\n"
    "buffer_load_dwordx4 v[112:115], v14, s[16:19], s82 offen offset:1024 sc1\n"
    "buffer_load_dwordx4 v[116:119], v14, s[16:19], s82 offen offset:3072 sc1\n"
    "s_add_u32 s82, s83, 0x3000\n"
    "buffer_load_dwordx4 v[120:123], v14, s[16:19], s82 offen offset:1024 sc1\n"
    "buffer_load_dwordx4 v[124:127], v14, s[16:19], s82 offen offset:3072 sc1\n"
    "s_waitcnt vmcnt(15)\n"
    "v_mfma_f32_32x32x16_bf16 v[128:143], a[192:195], v[64:67], 0\n"
    "v_mfma_f32_32x32x16_bf16 v[144:159], a[196:199], v[64:67], 0\n"
    "buffer_load_dwordx4 a[132:135], v22, s[16:19], s91 offen offset:1024 sc1\n"
    "s_waitcnt vmcnt(15)\n"
    "v_mfma_f32_32x32x16_bf16 v[128:143], a[200:203], v[68:71], v[128:143]\n"
    "buffer_load_dwordx4 a[136:139], v22, s[16:19], s91 offen offset:2048 sc1\n"
    "v_mfma_f32_32x32x16_bf16 v[144:159], a[204:207], v[68:71], v[144:159]\n"
    "buffer_load_dwordx4 a[140:143], v22, s[16:19], s91 offen offset:3072 sc1\n"
    "s_waitcnt vmcnt(16)\n"
    "v_mfma_f32_32x32x16_bf16 v[128:143], a[208:211], v[72:75], v[128:143]\n"
    "v_mfma_f32_32x32x16_bf16 v[144:159], a[212:215], v[72:75], v[144:159]\n"
    "s_add_u32 s91, s91, 0x1000\n"
    "buffer_load_dwordx4 a[144:147], v22, s[16:19], s91 offen sc1\n"
    "s_waitcnt vmcnt(16)\n"
    "v_mfma_f32_32x32x16_bf16 v[128:143], a[216:219], v[76:79], v[128:143]\n"
    "buffer_load_dwordx4 a[148:151], v22, s[16:19], s91 offen offset:1024 sc1\n"
    "v_mfma_f32_32x32x16_bf16 v[144:159], a[220:223], v[76:79], v[144:159]\n"
    "buffer_load_dwordx4 a[152:155], v22, s[16:19], s91 offen offset:2048 sc1\n"
    "s_waitcnt vmcnt(17)\n"
    "v_mfma_f32_32x32x16_bf16 v[128:143], a[224:227], v[80:83], v[128:143]\n"
    "v_mfma_f32_32x32x16_bf16 v[144:159], a[228:231], v[80:83], v[144:159]\n"
    "buffer_load_dwordx4 a[156:159], v22, s[16:19], s91 offen offset:3072 sc1\n"
    "s_waitcnt vmcnt(17)\n"
    "v_mfma_f32_32x32x16_bf16 v[128:143], a[232:235], v[84:87], v[128:143]\n"
    "s_add_u32 s91, s91, 0x1000\n"
    "buffer_load_dwordx4 a[160:163], v22, s[16:19], s91 offen sc1\n"
    "v_mfma_f32_32x32x16_bf16 v[144:159], a[236:239], v[84:87], v[144:159]\n"
    "buffer_load_dwordx4 a[164:167], v22, s[16:19], s91 offen offset:1024 sc1\n"
    "s_waitcnt vmcnt(18)\n"
    "v_mfma_f32_32x32x16_bf16 v[128:143], a[240:243], v[88:91], v[128:143]\n"
    "v_mfma_f32_32x32x16_bf16 v[144:159], a[244:247], v[88:91], v[144:159]\n"
    "buffer_load_dwordx4 a[168:171], v22, s[16:19], s91 offen offset:2048 sc1\n"
    "s_waitcnt vmcnt(18)\n"
    "v_mfma_f32_32x32x16_bf16 v[128:143], a[248:251], v[92:95], v[128:143]\n"
    "buffer_load_dwordx4 a[172:175], v22, s[16:19], s91 offen offset:3072 sc1\n"
    "v_mfma_f32_32x32x16_bf16 v[144:159], a[252:255], v[92:95], v[144:159]\n"
    "s_add_u32 s91, s91, 0x1000\n"
    "buffer_load_dwordx4 a[176:179], v22, s[16:19], s91 offen sc1\n"
    "s_waitcnt vmcnt(19)\n"
    "v_mfma_f32_32x32x16_bf16 v[160:175], a[192:195], v[96:99], 0\n"
    "v_mfma_f32_32x32x16_bf16 v[176:191], a[196:199], v[96:99], 0\n"
    "buffer_load_dwordx4 a[180:183], v22, s[16:19], s91 offen offset:1024 sc1\n"
    "s_waitcnt vmcnt(19)\n"
    "v_mfma_f32_32x32x16_bf16 v[160:175], a[200:203], v[100:103], v[160:175]\n"
    "buffer_load_dwordx4 a[184:187], v22, s[16:19], s91 offen offset:2048 sc1\n"
    "v_mfma_f32_32x32x16_bf16 v[176:191], a[204:207], v[100:103], v[176:191]\n"
    "ds_write_b128 v19, v[128:131]\n"
    "s_waitcnt vmcnt(19)\n"
    "v_mfma_f32_32x32x16_bf16 v[160:175], a[208:211], v[104:107], v[160:175]\n"
    "ds_write_b128 v19, v[132:135] offset:1152\n"
    "v_mfma_f32_32x32x16_bf16 v[176:191], a[212:215], v[104:107], v[176:191]\n"
    "ds_write_b128 v19, v[136:139] offset:2304\n"
    "s_waitcnt vmcnt(18)\n"
    "v_mfma_f32_32x32x16_bf16 v[160:175], a[216:219], v[108:111], v[160:175]\n"
    "ds_write_b128 v19, v[140:143] offset:3456\n"
    "v_mfma_f32_32x32x16_bf16 v[176:191], a[220:223], v[108:111], v[176:191]\n"
    "ds_write_b128 v19, v[144:147] offset:4608\n"
    "s_waitcnt vmcnt(17)\n"
    "v_mfma_f32_32x32x16_bf16 v[160:175], a[224:227], v[112:115], v[160:175]\n"
    "ds_write_b128 v19, v[148:151] offset:5760\n"
    "v_mfma_f32_32x32x16_bf16 v[176:191], a[228:231], v[112:115], v[176:191]\n"
    "ds_write_b128 v19, v[152:155] offset:6912\n"
    "s_waitcnt vmcnt(16)\n"
    "v_mfma_f32_32x32x16_bf16 v[160:175], a[232:235], v[116:119], v[160:175]\n"
    "ds_write_b128 v19, v[156:159] offset:8064\n"
    "s_waitcnt lgkmcnt(0)\n"
    "s_barrier\n"
    "v_mfma_f32_32x32x16_bf16 v[176:191], a[236:239], v[116:119], v[176:191]\n"
    "ds_read_b128 v[64:67], v28\n"
    "ds_read_b128 v[68:71], v28 offset:512\n"
    "ds_read_b128 v[72:75], v28 offset:18432\n"
    "ds_read_b128 v[76:79], v28 offset:18944\n"
    "ds_read_b128 v[80:83], v28 offset:36864\n"
    "ds_read_b128 v[84:87], v28 offset:37376\n"
    "s_waitcnt vmcnt(15)\n"
    "v_mfma_f32_32x32x16_bf16 v[160:175], a[240:243], v[120:123], v[160:175]\n"
    "ds_read_b128 v[88:91], v28 offset:55296\n"
    "ds_read_b128 v[92:95], v28 offset:55808\n"
    "v_mfma_f32_32x32x16_bf16 v[176:191], a[244:247], v[120:123], v[176:191]\n"
    "s_waitcnt vmcnt(14)\n"
    "v_mfma_f32_32x32x16_bf16 v[160:175], a[248:251], v[124:127], v[160:175]\n"
    "v_mfma_f32_32x32x16_bf16 v[176:191], a[252:255], v[124:127], v[176:191]\n"
    "buffer_store_dwordx2 v[12:13], v6, s[16:19], 0 offen sc1\n"
    "s_waitcnt lgkmcnt(7)\n"
    "v_pk_add_f32 v[30:31], v[30:31], v[64:65]\n"
    "v_pk_add_f32 v[32:33], v[32:33], v[66:67]\n"
    "s_waitcnt lgkmcnt(6)\n"
    "v_pk_add_f32 v[34:35], v[34:35], v[68:69]\n"
    "v_pk_add_f32 v[36:37], v[36:37], v[70:71]\n"
    "s_waitcnt lgkmcnt(5)\n"
    "v_pk_add_f32 v[30:31], v[30:31], v[72:73]\n"
    "v_pk_add_f32 v[32:33], v[32:33], v[74:75]\n"
    "s_waitcnt lgkmcnt(4)\n"
    "v_pk_add_f32 v[34:35], v[34:35], v[76:77]\n"
    "v_pk_add_f32 v[36:37], v[36:37], v[78:79]\n"
    "s_waitcnt lgkmcnt(3)\n"
    "v_pk_add_f32 v[30:31], v[30:31], v[80:81]\n"
    "v_pk_add_f32 v[32:33], v[32:33], v[82:83]\n"
    "s_waitcnt lgkmcnt(2)\n"
    "v_pk_add_f32 v[34:35], v[34:35], v[84:85]\n"
    "v_pk_add_f32 v[36:37], v[36:37], v[86:87]\n"
    "s_waitcnt lgkmcnt(1)\n"
    "v_pk_add_f32 v[30:31], v[30:31], v[88:89]\n"
    "v_pk_add_f32 v[32:33], v[32:33], v[90:91]\n"
    "s_waitcnt lgkmcnt(0)\n"
    "v_pk_add_f32 v[34:35], v[34:35], v[92:93]\n"
    "v_pk_add_f32 v[36:37], v[36:37], v[94:95]\n"
    "v_cvt_pk_bf16_f32 v136, v30, v31\n"
    "v_cvt_pk_bf16_f32 v137, v32, v33\n"
    "v_cvt_pk_bf16_f32 v138, v34, v35\n"
    "v_cvt_pk_bf16_f32 v139, v36, v37\n"
    "buffer_store_dwordx4 v[136:139], v50, s[16:19], s52 offen sc1\n"
    "v_dot2c_f32_bf16 v52, v136, v46\n"
    "v_dot2c_f32_bf16 v53, v136, v136\n"
    "v_dot2c_f32_bf16 v52, v137, v46\n"
    "v_dot2c_f32_bf16 v53, v137, v137\n"
    "v_dot2c_f32_bf16 v52, v138, v46\n"
    "v_dot2c_f32_bf16 v53, v138, v138\n"
    "v_dot2c_f32_bf16 v52, v139, v46\n"
    "v_dot2c_f32_bf16 v53, v139, v139\n"
    "s_nop 1\n"
    "v_add_f32 v52, v52, v52 row_ror:8 row_mask:0xf bank_mask:0xf\n"
    "v_add_f32 v53, v53, v53 row_ror:8 row_mask:0xf bank_mask:0xf\n"
    "s_nop 1\n"
    "v_permlane16_swap_b32 v52, v53\n"
    "v_add_f32 v52, v52, v53\n"
    "v_mov_b32 v53, v52\n"
    "s_nop 1\n"
    "v_permlane32_swap_b32 v52, v53\n"
    "v_add_f32 v52, v52, v53\n"
    "buffer_store_dword v52, v51, s[16:19], s54 offen sc1\n"
    "ds_write_b128 v19, v[160:163] offset:9216\n"
    "ds_write_b128 v19, v[164:167] offset:10368\n"
    "ds_write_b128 v19, v[168:171] offset:11520\n"
    "ds_write_b128 v19, v[172:175] offset:12672\n"
    "ds_write_b128 v19, v[176:179] offset:13824\n"
    "ds_write_b128 v19, v[180:183] offset:14976\n"
    "ds_write_b128 v19, v[184:187] offset:16128\n"
    "ds_write_b128 v19, v[188:191] offset:17280\n"
    "buffer_load_dwordx4 a[188:191], v22, s[16:19], s91 offen offset:3072 sc1\n"
    "s_mul_i32 s91, s76, 0xb000\n"
    "s_add_u32 s91, s91, 0x14a000\n"
    "buffer_load_dwordx4 v[192:195], v14, s[16:19], s91 offen sc1\n"
    "buffer_load_dwordx4 v[196:199], v14, s[16:19], s91 offen offset:1024 sc1\n"
    "buffer_load_dwordx4 v[200:203], v14, s[16:19], s91 offen offset:2048 sc1\n"
    "s_waitcnt lgkmcnt(0)\n"
    "s_barrier\n"
    "ds_read_b128 v[96:99], v28 offset:9216\n"
    "ds_read_b128 v[100:103], v28 offset:9728\n"
    "ds_read_b128 v[104:107], v28 offset:27648\n"
    "ds_read_b128 v[108:111], v28 offset:28160\n"
    "ds_read_b128 v[112:115], v28 offset:46080\n"
    "ds_read_b128 v[116:119], v28 offset:46592\n"
    "ds_read_b128 v[120:123], v28 offset:64512\n"
    "ds_read_b128 v[124:127], v28 offset:65024\n"
    "s_waitcnt vmcnt(4)\n"
    "s_barrier\n"
    "buffer_store_dwordx2 v[12:13], v18, s[16:19], s77 offen sc1\n"
    "s_add_u32 s77, s77, 0x20000\n"
    "s_waitcnt lgkmcnt(7)\n"
    "v_pk_add_f32 v[38:39], v[38:39], v[96:97]\n"
    "v_pk_add_f32 v[40:41], v[40:41], v[98:99]\n"
    "s_waitcnt lgkmcnt(6)\n"
    "v_pk_add_f32 v[42:43], v[42:43], v[100:101]\n"
    "v_pk_add_f32 v[44:45], v[44:45], v[102:103]\n"
    "s_waitcnt lgkmcnt(5)\n"
    "v_pk_add_f32 v[38:39], v[38:39], v[104:105]\n"
    "v_pk_add_f32 v[40:41], v[40:41], v[106:107]\n"
    "s_waitcnt lgkmcnt(4)\n"
    "v_pk_add_f32 v[42:43], v[42:43], v[108:109]\n"
    "v_pk_add_f32 v[44:45], v[44:45], v[110:111]\n"
    "s_waitcnt lgkmcnt(3)\n"
    "v_pk_add_f32 v[38:39], v[38:39], v[112:113]\n"
    "v_pk_add_f32 v[40:41], v[40:41], v[114:115]\n"
    "s_waitcnt lgkmcnt(2)\n"
    "v_pk_add_f32 v[42:43], v[42:43], v[116:117]\n"
    "v_pk_add_f32 v[44:45], v[44:45], v[118:119]\n"
    "s_waitcnt lgkmcnt(1)\n"
    "v_pk_add_f32 v[38:39], v[38:39], v[120:121]\n"
    "v_pk_add_f32 v[40:41], v[40:41], v[122:123]\n"
    "s_waitcnt lgkmcnt(0)\n"
    "v_pk_add_f32 v[42:43], v[42:43], v[124:125]\n"
    "v_pk_add_f32 v[44:45], v[44:45], v[126:127]\n"
    "v_cvt_pk_bf16_f32 v140, v38, v39\n"
    "v_cvt_pk_bf16_f32 v141, v40, v41\n"
    "v_cvt_pk_bf16_f32 v142, v42, v43\n"
    "v_cvt_pk_bf16_f32 v143, v44, v45\n"
    "buffer_store_dwordx4 v[140:143], v50, s[16:19], s53 offen sc1\n"
    "v_dot2c_f32_bf16 v54, v140, v46\n"
    "v_dot2c_f32_bf16 v55, v140, v140\n"
    "v_dot2c_f32_bf16 v54, v141, v46\n"
    "v_dot2c_f32_bf16 v55, v141, v141\n"
    "v_dot2c_f32_bf16 v54, v142, v46\n"
    "v_dot2c_f32_bf16 v55, v142, v142\n"
    "v_dot2c_f32_bf16 v54, v143, v46\n"
    "v_dot2c_f32_bf16 v55, v143, v143\n"
    "s_nop 1\n"
    "v_add_f32 v54, v54, v54 row_ror:8 row_mask:0xf bank_mask:0xf\n"
    "v_add_f32 v55, v55, v55 row_ror:8 row_mask:0xf bank_mask:0xf\n"
    "s_nop 1\n"
    "v_permlane16_swap_b32 v54, v55\n"
    "v_add_f32 v54, v54, v55\n"
    "v_mov_b32 v55, v54\n"
    "s_nop 1\n"
    "v_permlane32_swap_b32 v54, v55\n"
    "v_add_f32 v54, v54, v55\n"
    "buffer_store_dword v54, v51, s[16:19], s55 offen sc1\n"
    "buffer_load_dwordx4 v[204:207], v14, s[16:19], s91 offen offset:3072 sc1\n"
    "s_add_u32 s91, s91, 0x1000\n"
    "buffer_load_dwordx4 v[208:211], v14, s[16:19], s91 offen sc1\n"
    "buffer_load_dwordx4 v[212:215], v14, s[16:19], s91 offen offset:1024 sc1\n"
    "buffer_load_dwordx4 v[216:219], v14, s[16:19], s91 offen offset:2048 sc1\n"
    "v_and_b32 v2, 19, v1\n"
    "v_bfe_u32 v3, v1, 2, 1\n"
    "v_lshl_or_b32 v2, v3, 3, v2\n"
    "v_bfe_u32 v3, v1, 3, 1\n"
    "v_lshl_or_b32 v2, v3, 2, v2\n"
    "v_cmp_gt_u32 vcc, 32, v1\n"
    "v_lshlrev_b32 v2, 1, v2\n"
    "s_mul_i32 s53, s88, 0x7ff00000\n"
    "v_cndmask_b32 v2, v20, v2, vcc\n"
    "v_and_or_b32 v3, v16, v20, v2\n"
    "v_or_b32 v3, s53, v3\n"
    "s_lshl_b32 s52, s79, 6\n"
    "buffer_load_ushort v33, v2, s[36:39], s52 offen\n"
    "s_lshl_b32 s52, s80, 6\n"
    "buffer_load_ushort v37, v3, s[36:39], s52 offen\n"
    "s_lshl3_add_u32 s52, s50, s49\n"
    "s_lshl_b32 s52, s52, 10\n"
    "s_add_u32 s52, s52, 0x922000\n"
    "s_add_u32 s55, s52, 0x1000\n"
    "s_lshl_b32 s54, s50, 16\n"
    "s_lshl1_add_u32 s54, s66, s54\n"
    "s_add_u32 s54, s54, 0x722000\n"
    "v_mfma_f32_16x16x32_bf16 v[40:43], v[24:27], a[0:3], 0\n"
    "v_and_b32 v2, 0x180, v14\n"
    "v_lshl_or_b32 v2, v9, 3, v2\n"
    "v_mfma_f32_16x16x32_bf16 v[40:43], v[24:27], a[4:7], v[40:43]\n"
    "v_and_b32 v3, 16, v1\n"
    "v_lshl_or_b32 v2, v3, 2, v2\n"
    "v_mfma_f32_16x16x32_bf16 v[40:43], v[24:27], a[8:11], v[40:43]\n"
    "v_lshl_or_b32 v2, v21, 11, v2\n"
    "v_add_u32 v7, 0x10000, v2\n"
    "v_mfma_f32_16x16x32_bf16 v[40:43], v[24:27], a[12:15], v[40:43]\n"
    "s_add_u32 s53, s65, 0x24000\n"
    "v_lshl_or_b32 v20, s88, 14, v14\n"
    "v_xor_b32 v22, 0x4000, v20\n"
    "v_mfma_f32_16x16x32_bf16 v[40:43], v[24:27], a[16:19], v[40:43]\n"
    "v_add_u32 v23, s53, v14\n"
    "v_xor_b32 v29, 0x800, v23\n"
    "v_mfma_f32_16x16x32_bf16 v[40:43], v[24:27], a[20:23], v[40:43]\n"
    "v_mov_b32 v30, 0\n"
    "v_mov_b32 v31, 0\n"
    "v_mfma_f32_16x16x32_bf16 v[40:43], v[24:27], a[24:27], v[40:43]\n"
    "v_mov_b32 v34, 0\n"
    "v_mov_b32 v35, 0\n"
    "v_mfma_f32_16x16x32_bf16 v[40:43], v[24:27], a[28:31], v[40:43]\n"
    "v_mov_b32 v38, 0\n"
    "v_mov_b32 v39, 0\n"
    "s_add_u32 s78, s78, 0x20000\n"
    "s_not_b64 s[60:61], s[56:57]\n"
    "s_not_b64 s[62:63], s[56:57]\n"
    "s_buffer_load_dwordx4 s[60:63], s[16:19], s78 glc\n"
    "v_mfma_f32_16x16x32_bf16 v[40:43], v[24:27], a[32:35], v[40:43]\n"
    "v_mov_b32 v50, 0\n"
    "v_mov_b32 v51, 0\n"
    "v_mfma_f32_16x16x32_bf16 v[40:43], v[24:27], a[36:39], v[40:43]\n"
    "v_mfma_f32_16x16x32_bf16 v[40:43], v[24:27], a[40:43], v[40:43]\n"
    "v_mfma_f32_16x16x32_bf16 v[40:43], v[24:27], a[44:47], v[40:43]\n"
    "v_mfma_f32_16x16x32_bf16 v[40:43], v[24:27], a[48:51], v[40:43]\n"
    "v_mfma_f32_16x16x32_bf16 v[40:43], v[24:27], a[52:55], v[40:43]\n"
    "v_mfma_f32_16x16x32_bf16 v[40:43], v[24:27], a[56:59], v[40:43]\n"
    "v_mfma_f32_16x16x32_bf16 v[40:43], v[24:27], a[60:63], v[40:43]\n"
    "v_mfma_f32_16x16x32_bf16 v[40:43], v[24:27], a[64:67], v[40:43]\n"
    "v_mfma_f32_16x16x32_bf16 v[40:43], v[24:27], a[68:71], v[40:43]\n"
    "v_mfma_f32_16x16x32_bf16 v[40:43], v[24:27], a[72:75], v[40:43]\n"
    "v_mfma_f32_16x16x32_bf16 v[40:43], v[24:27], a[76:79], v[40:43]\n"
    "s_waitcnt vmcnt(6)\n"
    "s_barrier\n"
    "buffer_store_dwordx2 v[12:13], v18, s[16:19], s77 offen sc1\n"
    "s_add_u32 s77, s77, 0x20000\n"
    "s_xor_b64 s[92:93], s[60:61], s[56:57]\n"
    "s_xor_b64 vcc, s[62:63], s[56:57]\n"
    "s_or_b64 s[92:93], s[92:93], vcc\n"
    "s_cbranch_scc0 .Lhh_s5d\n"
    "s_mov_b32 s58, 48\n"
    ".Lhh_s5a:\n"
    "s_buffer_load_dwordx4 s[60:63], s[16:19], s78 glc\n"
    "s_sleep 1\n"
    "s_xor_b64 s[92:93], s[60:61], s[56:57]\n"
    "s_xor_b64 vcc, s[62:63], s[56:57]\n"
    "s_or_b64 s[92:93], s[92:93], vcc\n"
    "s_cbranch_scc0 .Lhh_s5d\n"
    "s_sleep 1\n"
    "s_xor_b64 s[92:93], s[60:61], s[56:57]\n"
    "s_xor_b64 vcc, s[62:63], s[56:57]\n"
    "s_or_b64 s[92:93], s[92:93], vcc\n"
    "s_cbranch_scc0 .Lhh_s5d\n"
    "s_sleep 1\n"
    "s_xor_b64 s[92:93], s[60:61], s[56:57]\n"
    "s_xor_b64 vcc, s[62:63], s[56:57]\n"
    "s_or_b64 s[92:93], s[92:93], vcc\n"
    "s_cbranch_scc0 .Lhh_s5d\n"
    "s_sub_u32 s58, s58, 1\n"
    "s_cbranch_scc0 .Lhh_s5a\n"
    "s_branch .Lhh_s5\n"
    ".Lhh_s5d:\n"
    "s_add_u32 m0, s75, 0x10000\n"
    "s_nop 0\n"
    "buffer_load_dwordx4 v14, s[16:19], s52 offen sc1 lds\n"
    "s_lshl1_add_u32 m0, s66, 0\n"
    "s_nop 0\n"
    "buffer_load_dwordx4 v14, s[16:19], s54 offen sc1 lds\n"
    "buffer_load_dwordx4 v14, s[16:19], s54 offen offset:1024 sc1 lds\n"
    "buffer_load_dwordx4 v14, s[16:19], s54 offen offset:2048 sc1 lds\n"
    "buffer_load_dwordx4 v14, s[16:19], s54 offen offset:3072 sc1 lds\n"
    "s_lshl1_add_u32 m0, s66, 0x1000\n"
    "s_add_u32 s82, s54, 0x1000\n"
    "buffer_load_dwordx4 v14, s[16:19], s82 offen sc1 lds\n"
    "buffer_load_dwordx4 v14, s[16:19], s82 offen offset:1024 sc1 lds\n"
    "buffer_load_dwordx4 v14, s[16:19], s82 offen offset:2048 sc1 lds\n"
    "buffer_load_dwordx4 v14, s[16:19], s82 offen offset:3072 sc1 lds\n"
    "v_mfma_f32_16x16x32_bf16 v[40:43], v[24:27], a[80:83], v[40:43]\n"
    "buffer_load_dwordx4 v[220:223], v14, s[16:19], s91 offen offset:3072 sc1\n"
    "v_mfma_f32_16x16x32_bf16 v[40:43], v[24:27], a[84:87], v[40:43]\n"
    "v_mfma_f32_16x16x32_bf16 v[40:43], v[24:27], a[88:91], v[40:43]\n"
    "v_mfma_f32_16x16x32_bf16 v[40:43], v[24:27], a[92:95], v[40:43]\n"
    "s_add_u32 s91, s91, 0x1000\n"
    "buffer_load_dwordx4 v[224:227], v14, s[16:19], s91 offen sc1\n"
    "v_mfma_f32_16x16x32_bf16 v[40:43], v[24:27], a[96:99], v[40:43]\n"
    "v_mfma_f32_16x16x32_bf16 v[40:43], v[24:27], a[100:103], v[40:43]\n"
    "v_mfma_f32_16x16x32_bf16 v[40:43], v[24:27], a[104:107], v[40:43]\n"
    "v_mfma_f32_16x16x32_bf16 v[40:43], v[24:27], a[108:111], v[40:43]\n"
    "buffer_load_dwordx4 v[228:231], v14, s[16:19], s91 offen offset:1024 sc1\n"
    "v_mfma_f32_16x16x32_bf16 v[40:43], v[24:27], a[112:115], v[40:43]\n"
    "v_mfma_f32_16x16x32_bf16 v[40:43], v[24:27], a[116:119], v[40:43]\n"
    "v_mfma_f32_16x16x32_bf16 v[40:43], v[24:27], a[120:123], v[40:43]\n"
    "buffer_load_dwordx4 v[232:235], v14, s[16:19], s91 offen offset:2048 sc1\n"
    "v_mfma_f32_16x16x32_bf16 v[40:43], v[24:27], a[124:127], v[40:43]\n"
    "v_mfma_f32_16x16x32_bf16 v[44:47], v[24:27], a[128:131], 0\n"
    "v_mfma_f32_16x16x32_bf16 v[44:47], v[24:27], a[132:135], v[44:47]\n"
    "v_mfma_f32_16x16x32_bf16 v[44:47], v[24:27], a[136:139], v[44:47]\n"
    "buffer_load_dwordx4 v[236:239], v14, s[16:19], s91 offen offset:3072 sc1\n"
    "v_mfma_f32_16x16x32_bf16 v[44:47], v[24:27], a[140:143], v[44:47]\n"
    "v_mfma_f32_16x16x32_bf16 v[44:47], v[24:27], a[144:147], v[44:47]\n"
    "v_mfma_f32_16x16x32_bf16 v[44:47], v[24:27], a[148:151], v[44:47]\n"
    "s_add_u32 s91, s91, 0x1000\n"
    "buffer_load_dwordx4 v[240:243], v14, s[16:19], s91 offen sc1\n"
    "v_mfma_f32_16x16x32_bf16 v[44:47], v[24:27], a[152:155], v[44:47]\n"
    "v_mfma_f32_16x16x32_bf16 v[44:47], v[24:27], a[156:159], v[44:47]\n"
    "v_mfma_f32_16x16x32_bf16 v[44:47], v[24:27], a[160:163], v[44:47]\n"
    "v_mfma_f32_16x16x32_bf16 v[44:47], v[24:27], a[164:167], v[44:47]\n"
    "buffer_load_dwordx4 v[244:247], v14, s[16:19], s91 offen offset:1024 sc1\n"
    "v_mfma_f32_16x16x32_bf16 v[44:47], v[24:27], a[168:171], v[44:47]\n"
    "v_mfma_f32_16x16x32_bf16 v[44:47], v[24:27], a[172:175], v[44:47]\n"
    "v_mfma_f32_16x16x32_bf16 v[44:47], v[24:27], a[176:179], v[44:47]\n"
    "buffer_load_dwordx4 v[248:251], v14, s[16:19], s91 offen offset:2048 sc1\n"
    "v_mfma_f32_16x16x32_bf16 v[44:47], v[24:27], a[180:183], v[44:47]\n"
    "v_mfma_f32_16x16x32_bf16 v[44:47], v[24:27], a[184:187], v[44:47]\n"
    "v_mfma_f32_16x16x32_bf16 v[44:47], v[24:27], a[188:191], v[44:47]\n"
    "v_cmp_eq_u32 vcc, 0, v21\n"
    "v_cvt_pk_bf16_f32 v41, v40, v40\n"
    "v_and_b32 v43, 0xffff0000, v41\n"
    "v_sub_f32 v43, v40, v43\n"
    "v_cvt_pk_bf16_f32 v42, v43, v43\n"
    "v_cndmask_b32 v32, v42, v41, vcc\n"
    "s_nop 1\n"
    "v_cvt_pk_bf16_f32 v45, v44, v44\n"
    "v_and_b32 v47, 0xffff0000, v45\n"
    "v_sub_f32 v47, v44, v47\n"
    "v_cvt_pk_bf16_f32 v46, v47, v47\n"
    "v_cndmask_b32 v36, v46, v45, vcc\n"
    "s_waitcnt vmcnt(16)\n"
    "s_barrier\n"
    "ds_read_b64 v[68:69], v7\n"
    "ds_read_b64 v[70:71], v7 offset:512\n"
    "ds_read_b64 v[72:73], v7 offset:1024\n"
    "ds_read_b64 v[74:75], v7 offset:1536\n"
    "s_waitcnt lgkmcnt(0)\n"
    "v_add_f32 v68, v68, v72\n"
    "v_add_f32 v69, v69, v73\n"
    "v_add_f32 v70, v70, v74\n"
    "v_add_f32 v71, v71, v75\n"
    "v_add_f32 v2, v68, v70\n"
    "v_add_f32 v3, v69, v71\n"
    "s_waitcnt vmcnt(8)\n"
    "s_barrier\n"
    "ds_read_b128 v[116:119], v20\n"
    "ds_read_b128 v[120:123], v20 offset:1024\n"
    "ds_read_b128 v[124:127], v20 offset:2048\n"
    "ds_read_b128 v[128:131], v20 offset:3072\n"
    "ds_read_b128 v[132:135], v20 offset:4096\n"
    "s_waitcnt lgkmcnt(4)\n"
    "v_mfma_f32_32x32x16_bf16 v[84:99], a[128:131], v[116:119], 0\n"
    "s_waitcnt lgkmcnt(3)\n"
    "ds_read_b128 v[24:27], v20 offset:5120\n"
    "s_add_u32 s78, s78, 0x20000\n"
    "buffer_load_dwordx2 v[10:11], v15, s[16:19], s78 offen sc1\n"
    "v_mov_b32 v4, v2\n"
    "v_mov_b32 v5, v3\n"
    "v_mfma_f32_32x32x16_bf16 v[84:99], a[132:135], v[120:123], v[84:99]\n"
    "s_waitcnt lgkmcnt(3)\n"
    "ds_read_b128 v[116:119], v20 offset:6144\n"
    "buffer_load_dwordx4 v[252:255], v14, s[16:19], s91 offen offset:3072 sc1\n"
    "v_permlane32_swap_b32 v2, v4\n"
    "v_permlane32_swap_b32 v3, v5\n"
    "v_mfma_f32_32x32x16_bf16 v[84:99], a[136:139], v[124:127], v[84:99]\n"
    "s_waitcnt lgkmcnt(3)\n"
    "ds_read_b128 v[120:123], v20 offset:7168\n"
    "s_add_u32 s91, s91, 0x1000\n"
    "buffer_load_dwordx4 a[192:195], v14, s[16:19], s91 offen sc1\n"
    "v_add_f32 v2, v2, v4\n"
    "v_add_f32 v3, v3, v5\n"
    "v_mul_f32 v2, 0xbb000000, v2\n"
    "v_mul_f32 v3, 0x3b000000, v3\n"
    "v_mfma_f32_32x32x16_bf16 v[84:99], a[140:143], v[128:131], v[84:99]\n"
    "s_waitcnt lgkmcnt(3)\n"
    "ds_read_b128 v[124:127], v20 offset:8192\n"
    "v_cvt_pk_bf16_f32 v48, v2, v2\n"
    "v_fma_f32 v3, -v2, v2, v3\n"
    "v_and_b32 v6, 0xffff0000, v48\n"
    "v_max_f32 v3, 0, v3\n"
    "v_sub_f32 v6, v2, v6\n"
    "v_mfma_f32_32x32x16_bf16 v[84:99], a[144:147], v[132:135], v[84:99]\n"
    "s_waitcnt lgkmcnt(3)\n"
    "ds_read_b128 v[128:131], v20 offset:9216\n"
    "buffer_load_dwordx4 a[196:199], v14, s[16:19], s91 offen offset:1024 sc1\n"
    "v_add_f32 v3, 0x3727c5ac, v3\n"
    "v_cvt_pk_bf16_f32 v48, v2, v6\n"
    "v_sqrt_f32 v102, v3\n"
    "v_mfma_f32_32x32x16_bf16 v[84:99], a[148:151], v[24:27], v[84:99]\n"
    "s_waitcnt lgkmcnt(3)\n"
    "ds_read_b128 v[132:135], v20 offset:10240\n"
    "buffer_load_dwordx4 a[200:203], v14, s[16:19], s91 offen offset:2048 sc1\n"
    "v_rsq_f32 v5, v3\n"
    "v_cvt_pk_bf16_f32 v49, v102, v102\n"
    "v_mul_f32 v6, v5, v5\n"
    "v_mfma_f32_32x32x16_bf16 v[84:99], a[152:155], v[116:119], v[84:99]\n"
    "s_waitcnt lgkmcnt(3)\n"
    "ds_read_b128 v[24:27], v20 offset:11264\n"
    "v_and_b32 v4, 0xffff0000, v49\n"
    "v_mul_f32 v100, 0xc0135761, v5\n"
    "v_sub_f32 v4, v102, v4\n"
    "v_mul_f32 v6, v6, v5\n"
    "v_cvt_pk_bf16_f32 v49, v102, v4\n"
    "v_mfma_f32_32x32x16_bf16 v[84:99], a[156:159], v[120:123], v[84:99]\n"
    "s_waitcnt lgkmcnt(3)\n"
    "ds_read_b128 v[116:119], v20 offset:12288\n"
    "buffer_load_dwordx4 a[204:207], v14, s[16:19], s91 offen offset:3072 sc1\n"
    "v_mul_f32 v101, 0xbdd2d3e8, v6\n"
    "v_lshl_or_b32 v33, v33, 16, v33\n"
    "v_lshl_or_b32 v37, v37, 16, v37\n"
    "v_mfma_f32_32x32x16_bf16 v[84:99], a[160:163], v[124:127], v[84:99]\n"
    "s_waitcnt lgkmcnt(3)\n"
    "ds_read_b128 v[120:123], v20 offset:13312\n"
    "s_add_u32 s91, s91, 0x1000\n"
    "buffer_load_dwordx4 a[208:211], v14, s[16:19], s91 offen sc1\n"
    "v_mfma_f32_32x32x16_bf16 v[84:99], a[164:167], v[128:131], v[84:99]\n"
    "s_waitcnt lgkmcnt(3)\n"
    "ds_read_b128 v[124:127], v20 offset:14336\n"
    "v_mfma_f32_32x32x16_bf16 v[84:99], a[168:171], v[132:135], v[84:99]\n"
    "s_waitcnt lgkmcnt(3)\n"
    "ds_read_b128 v[128:131], v20 offset:15360\n"
    "v_mfma_f32_32x32x16_bf16 v[84:99], a[172:175], v[24:27], v[84:99]\n"
    "s_waitcnt lgkmcnt(3)\n"
    "ds_read_b128 v[132:135], v20\n"
    "v_mfma_f32_32x32x16_bf16 v[84:99], a[176:179], v[116:119], v[84:99]\n"
    "s_waitcnt lgkmcnt(3)\n"
    "ds_read_b128 v[24:27], v20 offset:1024\n"
    "v_mfma_f32_32x32x16_bf16 v[84:99], a[180:183], v[120:123], v[84:99]\n"
    "s_waitcnt lgkmcnt(3)\n"
    "ds_read_b128 v[116:119], v20 offset:2048\n"
    "v_mfma_f32_32x32x16_bf16 v[84:99], a[184:187], v[124:127], v[84:99]\n"
    "s_waitcnt lgkmcnt(3)\n"
    "ds_read_b128 v[120:123], v20 offset:3072\n"
    "s_waitcnt vmcnt(6)\n"
    "v_cmp_ne_u64 vcc, s[56:57], v[10:11]\n"
    "s_nop 0\n"
    "s_cbranch_vccnz .Lhh_p6\n"
    ".Lhh_p6d:\n"
    "v_mfma_f32_32x32x16_bf16 v[84:99], a[188:191], v[128:131], v[84:99]\n"
    "s_waitcnt lgkmcnt(3)\n"
    "ds_read_b128 v[124:127], v20 offset:4096\n"
    "s_add_u32 m0, s75, 0x11000\n"
    "s_nop 0\n"
    "buffer_load_dwordx4 v14, s[16:19], s55 offen sc1 lds\n"
    "v_mfma_f32_32x32x16_bf16 v[84:99], v[36:39], v[48:51], v[84:99]\n"
    "v_mfma_f32_32x32x16_bf16 v[52:67], a[0:3], v[132:135], 0\n"
    "s_waitcnt lgkmcnt(3)\n"
    "ds_read_b128 v[128:131], v20 offset:5120\n"
    "s_lshl1_add_u32 m0, s66, 0x8000\n"
    "s_add_u32 s82, s54, 0x8000\n"
    "buffer_load_dwordx4 v14, s[16:19], s82 offen sc1 lds\n"
    "v_mfma_f32_32x32x16_bf16 v[52:67], a[4:7], v[24:27], v[52:67]\n"
    "s_waitcnt lgkmcnt(3)\n"
    "ds_read_b128 v[132:135], v20 offset:6144\n"
    "buffer_load_dwordx4 v14, s[16:19], s82 offen offset:1024 sc1 lds\n"
    "v_mfma_f32_32x32x16_bf16 v[52:67], a[8:11], v[116:119], v[52:67]\n"
    "s_waitcnt lgkmcnt(3)\n"
    "ds_read_b128 v[24:27], v20 offset:7168\n"
    "buffer_load_dwordx4 v14, s[16:19], s82 offen offset:2048 sc1 lds\n"
    "ds_write_b128 v23, v[92:95]\n"
    "v_mfma_f32_32x32x16_bf16 v[52:67], a[12:15], v[120:123], v[52:67]\n"
    "s_waitcnt lgkmcnt(4)\n"
    "ds_read_b128 v[116:119], v20 offset:8192\n"
    "buffer_load_dwordx4 v14, s[16:19], s82 offen offset:3072 sc1 lds\n"
    "ds_write_b128 v23, v[96:99] offset:1024\n"
    "v_mfma_f32_32x32x16_bf16 v[52:67], a[16:19], v[124:127], v[52:67]\n"
    "s_waitcnt lgkmcnt(5)\n"
    "ds_read_b128 v[120:123], v20 offset:9216\n"
    "s_lshl1_add_u32 m0, s66, 0x9000\n"
    "s_add_u32 s82, s54, 0x9000\n"
    "buffer_load_dwordx4 v14, s[16:19], s82 offen sc1 lds\n"
    "s_waitcnt lgkmcnt(1)\n"
    "s_barrier\n"
    "v_mfma_f32_32x32x16_bf16 v[52:67], a[20:23], v[128:131], v[52:67]\n"
    "ds_read_b128 v[124:127], v20 offset:10240\n"
    "buffer_load_dwordx4 v14, s[16:19], s82 offen offset:1024 sc1 lds\n"
    "ds_read_b128 v[108:111], v29\n"
    "ds_read_b128 v[112:115], v29 offset:1024\n"
    "v_mfma_f32_32x32x16_bf16 v[52:67], a[24:27], v[132:135], v[52:67]\n"
    "ds_read_b128 v[128:131], v20 offset:11264\n"
    "buffer_load_dwordx4 v14, s[16:19], s82 offen offset:2048 sc1 lds\n"
    "v_mfma_f32_32x32x16_bf16 v[52:67], a[28:31], v[24:27], v[52:67]\n"
    "ds_read_b128 v[132:135], v20 offset:12288\n"
    "buffer_load_dwordx4 v14, s[16:19], s82 offen offset:3072 sc1 lds\n"
    "v_mfma_f32_32x32x16_bf16 v[52:67], a[32:35], v[116:119], v[52:67]\n"
    "s_waitcnt lgkmcnt(5)\n"
    "ds_read_b128 v[24:27], v20 offset:13312\n"
    "v_mfma_f32_32x32x16_bf16 v[52:67], a[36:39], v[120:123], v[52:67]\n"
    "s_waitcnt lgkmcnt(5)\n"
    "ds_read_b128 v[116:119], v20 offset:14336\n"
    "s_waitcnt lgkmcnt(5)\n"
    "v_add_f32 v108, v108, v84\n"
    "v_add_f32 v109, v109, v85\n"
    "v_add_f32 v110, v110, v86\n"
    "v_add_f32 v111, v111, v87\n"
    "s_waitcnt lgkmcnt(4)\n"
    "v_add_f32 v112, v112, v88\n"
    "v_mfma_f32_32x32x16_bf16 v[52:67], a[40:43], v[124:127], v[52:67]\n"
    "s_waitcnt lgkmcnt(3)\n"
    "ds_read_b128 v[120:123], v20 offset:15360\n"
    "v_add_f32 v113, v113, v89\n"
    "v_add_f32 v114, v114, v90\n"
    "v_add_f32 v115, v115, v91\n"
    "v_mul_f32 v40, v108, v108\n"
    "v_mul_f32 v41, v109, v109\n"
    "v_mfma_f32_32x32x16_bf16 v[52:67], a[44:47], v[128:131], v[52:67]\n"
    "s_waitcnt lgkmcnt(3)\n"
    "ds_read_b128 v[124:127], v22\n"
    "v_fma_f32 v40, v40, v101, v100\n"
    "v_mul_f32 v42, v110, v110\n"
    "v_fma_f32 v41, v41, v101, v100\n"
    "v_mul_f32 v43, v111, v111\n"
    "v_mul_f32 v40, v40, v108\n"
    "v_mfma_f32_32x32x16_bf16 v[52:67], a[48:51], v[132:135], v[52:67]\n"
    "s_waitcnt lgkmcnt(3)\n"
    "ds_read_b128 v[128:131], v22 offset:1024\n"
    "v_fma_f32 v42, v42, v101, v100\n"
    "v_mul_f32 v44, v112, v112\n"
    "v_mul_f32 v41, v41, v109\n"
    "v_fma_f32 v43, v43, v101, v100\n"
    "v_mul_f32 v45, v113, v113\n"
    "v_mfma_f32_32x32x16_bf16 v[52:67], a[52:55], v[24:27], v[52:67]\n"
    "s_waitcnt lgkmcnt(3)\n"
    "ds_read_b128 v[132:135], v22 offset:2048\n"
    "buffer_load_dwordx4 a[212:215], v14, s[16:19], s91 offen offset:1024 sc1\n"
    "v_mul_f32 v42, v42, v110\n"
    "v_exp_f32 v40, v40\n"
    "v_fma_f32 v44, v44, v101, v100\n"
    "v_mfma_f32_32x32x16_bf16 v[52:67], a[56:59], v[116:119], v[52:67]\n"
    "s_waitcnt lgkmcnt(3)\n"
    "ds_read_b128 v[24:27], v22 offset:3072\n"
    "buffer_load_dwordx4 a[216:219], v14, s[16:19], s91 offen offset:2048 sc1\n"
    "v_mul_f32 v46, v114, v114\n"
    "v_mul_f32 v43, v43, v111\n"
    "v_exp_f32 v41, v41\n"
    "v_mfma_f32_32x32x16_bf16 v[52:67], a[60:63], v[120:123], v[52:67]\n"
    "s_waitcnt lgkmcnt(3)\n"
    "ds_read_b128 v[116:119], v22 offset:4096\n"
    "v_fma_f32 v45, v45, v101, v100\n"
    "v_mul_f32 v47, v115, v115\n"
    "v_fma_f32 v40, v40, v102, v102\n"
    "v_mul_f32 v44, v44, v112\n"
    "v_mfma_f32_32x32x16_bf16 v[52:67], a[64:67], v[124:127], v[52:67]\n"
    "s_waitcnt lgkmcnt(3)\n"
    "ds_read_b128 v[120:123], v22 offset:5120\n"
    "buffer_load_dwordx4 a[220:223], v14, s[16:19], s91 offen offset:3072 sc1\n"
    "v_exp_f32 v42, v42\n"
    "v_fma_f32 v46, v46, v101, v100\n"
    "v_fma_f32 v41, v41, v102, v102\n"
    "v_mfma_f32_32x32x16_bf16 v[52:67], a[68:71], v[128:131], v[52:67]\n"
    "s_waitcnt lgkmcnt(3)\n"
    "ds_read_b128 v[124:127], v22 offset:6144\n"
    "v_mul_f32 v45, v45, v113\n"
    "v_exp_f32 v43, v43\n"
    "v_fma_f32 v47, v47, v101, v100\n"
    "v_mfma_f32_32x32x16_bf16 v[52:67], a[72:75], v[132:135], v[52:67]\n"
    "s_waitcnt lgkmcnt(3)\n"
    "ds_read_b128 v[128:131], v22 offset:7168\n"
    "s_add_u32 s91, s91, 0x1000\n"
    "buffer_load_dwordx4 a[224:227], v14, s[16:19], s91 offen sc1\n"
    "v_rcp_f32 v40, v40\n"
    "v_fma_f32 v42, v42, v102, v102\n"
    "v_mul_f32 v46, v46, v114\n"
    "v_mfma_f32_32x32x16_bf16 v[52:67], a[76:79], v[24:27], v[52:67]\n"
    "s_waitcnt lgkmcnt(3)\n"
    "ds_read_b128 v[132:135], v22 offset:8192\n"
    "buffer_load_dwordx4 a[228:231], v14, s[16:19], s91 offen offset:1024 sc1\n"
    "v_exp_f32 v44, v44\n"
    "v_rcp_f32 v41, v41\n"
    "v_mfma_f32_32x32x16_bf16 v[52:67], a[80:83], v[116:119], v[52:67]\n"
    "s_waitcnt lgkmcnt(3)\n"
    "ds_read_b128 v[24:27], v22 offset:9216\n"
    "v_fma_f32 v43, v43, v102, v102\n"
    "v_mul_f32 v47, v47, v115\n"
    "v_exp_f32 v45, v45\n"
    "v_mul_f32 v40, v40, v108\n"
    "v_mfma_f32_32x32x16_bf16 v[52:67], a[84:87], v[120:123], v[52:67]\n"
    "s_waitcnt lgkmcnt(3)\n"
    "ds_read_b128 v[116:119], v22 offset:10240\n"
    "buffer_load_dwordx4 a[232:235], v14, s[16:19], s91 offen offset:2048 sc1\n"
    "v_rcp_f32 v42, v42\n"
    "v_fma_f32 v44, v44, v102, v102\n"
    "v_mfma_f32_32x32x16_bf16 v[52:67], a[88:91], v[124:127], v[52:67]\n"
    "s_waitcnt lgkmcnt(3)\n"
    "ds_read_b128 v[120:123], v22 offset:11264\n"
    "v_exp_f32 v46, v46\n"
    "v_mul_f32 v41, v41, v109\n"
    "v_rcp_f32 v43, v43\n"
    "v_mfma_f32_32x32x16_bf16 v[52:67], a[92:95], v[128:131], v[52:67]\n"
    "s_waitcnt lgkmcnt(3)\n"
    "ds_read_b128 v[124:127], v22 offset:12288\n"
    "buffer_load_dwordx4 a[236:239], v14, s[16:19], s91 offen offset:3072 sc1\n"
    "v_fma_f32 v45, v45, v102, v102\n"
    "v_exp_f32 v47, v47\n"
    "v_mul_f32 v42, v42, v110\n"
    "v_mfma_f32_32x32x16_bf16 v[52:67], a[96:99], v[132:135], v[52:67]\n"
    "s_waitcnt lgkmcnt(3)\n"
    "ds_read_b128 v[128:131], v22 offset:13312\n"
    "s_add_u32 s91, s91, 0x1000\n"
    "buffer_load_dwordx4 a[240:243], v14, s[16:19], s91 offen sc1\n"
    "v_rcp_f32 v44, v44\n"
    "v_fma_f32 v46, v46, v102, v102\n"
    "v_cvt_pk_bf16_f32 v108, v40, v41\n"
    "v_mfma_f32_32x32x16_bf16 v[52:67], a[100:103], v[24:27], v[52:67]\n"
    "s_waitcnt lgkmcnt(3)\n"
    "ds_read_b128 v[132:135], v22 offset:14336\n"
    "v_mul_f32 v43, v43, v111\n"
    "v_rcp_f32 v45, v45\n"
    "v_fma_f32 v47, v47, v102, v102\n"
    "v_mul_f32 v44, v44, v112\n"
    "v_mfma_f32_32x32x16_bf16 v[52:67], a[104:107], v[116:119], v[52:67]\n"
    "s_waitcnt lgkmcnt(3)\n"
    "ds_read_b128 v[24:27], v22 offset:15360\n"
    "buffer_load_dwordx4 a[244:247], v14, s[16:19], s91 offen offset:1024 sc1\n"
    "v_rcp_f32 v46, v46\n"
    "v_cvt_pk_bf16_f32 v109, v42, v43\n"
    "v_mul_f32 v45, v45, v113\n"
    "s_waitcnt vmcnt(9)\n"
    "s_barrier\n"
    "v_mfma_f32_32x32x16_bf16 v[52:67], a[108:111], v[120:123], v[52:67]\n"
    "s_waitcnt lgkmcnt(3)\n"
    "ds_read_b128 v[116:119], v20 offset:32768\n"
    "ds_read_b64 v[68:69], v7 offset:4096\n"
    "ds_read_b64 v[70:71], v7 offset:4608\n"
    "ds_read_b64 v[72:73], v7 offset:5120\n"
    "ds_read_b64 v[74:75], v7 offset:5632\n"
    "v_mfma_f32_32x32x16_bf16 v[52:67], a[112:115], v[124:127], v[52:67]\n"
    "s_waitcnt lgkmcnt(7)\n"
    "ds_read_b128 v[120:123], v20 offset:33792\n"
    "buffer_load_dwordx4 a[248:251], v14, s[16:19], s91 offen offset:2048 sc1\n"
    "v_rcp_f32 v47, v47\n"
    "v_mul_f32 v46, v46, v114\n"
    "v_cvt_pk_bf16_f32 v110, v44, v45\n"
    "v_mfma_f32_32x32x16_bf16 v[52:67], a[116:119], v[128:131], v[52:67]\n"
    "s_waitcnt lgkmcnt(7)\n"
    "ds_read_b128 v[124:127], v20 offset:34816\n"
    "buffer_load_dwordx4 a[252:255], v14, s[16:19], s91 offen offset:3072 sc1\n"
    "s_waitcnt lgkmcnt(3)\n"
    "v_add_f32 v68, v68, v72\n"
    "v_add_f32 v69, v69, v73\n"
    "s_waitcnt lgkmcnt(2)\n"
    "v_add_f32 v70, v70, v74\n"
    "v_add_f32 v71, v71, v75\n"
    "v_mfma_f32_32x32x16_bf16 v[52:67], a[120:123], v[132:135], v[52:67]\n"
    "ds_read_b128 v[128:131], v20 offset:35840\n"
    "v_add_f32 v2, v68, v70\n"
    "v_add_f32 v3, v69, v71\n"
    "v_mul_f32 v47, v47, v115\n"
    "v_cvt_pk_bf16_f32 v111, v46, v47\n"
    "v_mov_b32 v4, v2\n"
    "v_mfma_f32_32x32x16_bf16 v[52:67], a[124:127], v[24:27], v[52:67]\n"
    "ds_read_b128 v[132:135], v20 offset:36864\n"
    "s_add_u32 s91, s91, 0x1000\n"
    "buffer_load_dwordx4 v[144:147], v14, s[16:19], s91 offen sc1\n"
    "buffer_store_dwordx4 v[108:111], v16, s[16:19], s85 offen sc1\n"
    "s_add_u32 s85, s85, 0x16000\n"
    "v_mov_b32 v5, v3\n"
    "v_mfma_f32_32x32x16_bf16 v[52:67], v[32:35], v[48:51], v[52:67]\n"
    "v_permlane32_swap_b32 v2, v4\n"
    "v_permlane32_swap_b32 v3, v5\n"
    "v_add_f32 v2, v2, v4\n"
    "v_add_f32 v3, v3, v5\n"
    "v_mfma_f32_32x32x16_bf16 v[68:83], a[0:3], v[116:119], 0\n"
    "ds_read_b128 v[24:27], v20 offset:37888\n"
    "buffer_load_dwordx4 v[148:151], v14, s[16:19], s91 offen offset:1024 sc1\n"
    "v_mul_f32 v2, 0xbb000000, v2\n"
    "v_mul_f32 v3, 0x3b000000, v3\n"
    "v_cvt_pk_bf16_f32 v50, v2, v2\n"
    "v_fma_f32 v3, -v2, v2, v3\n"
    "v_mfma_f32_32x32x16_bf16 v[84:99], a[128:131], v[116:119], 0\n"
    "s_waitcnt lgkmcnt(4)\n"
    "v_and_b32 v6, 0xffff0000, v50\n"
    "v_max_f32 v3, 0, v3\n"
    "v_sub_f32 v6, v2, v6\n"
    "v_add_f32 v3, 0x3727c5ac, v3\n"
    "v_cvt_pk_bf16_f32 v50, v2, v6\n"
    "v_mfma_f32_32x32x16_bf16 v[68:83], a[4:7], v[120:123], v[68:83]\n"
    "ds_read_b128 v[116:119], v20 offset:38912\n"
    "v_sqrt_f32 v106, v3\n"
    "v_rsq_f32 v5, v3\n"
    "v_cvt_pk_bf16_f32 v51, v106, v106\n"
    "v_mfma_f32_32x32x16_bf16 v[84:99], a[132:135], v[120:123], v[84:99]\n"
    "s_waitcnt lgkmcnt(4)\n"
    "v_mul_f32 v6, v5, v5\n"
    "v_and_b32 v4, 0xffff0000, v51\n"
    "v_mul_f32 v104, 0xc0135761, v5\n"
    "v_sub_f32 v4, v106, v4\n"
    "v_mul_f32 v6, v6, v5\n"
    "v_cvt_pk_bf16_f32 v51, v106, v4\n"
    "v_mfma_f32_32x32x16_bf16 v[68:83], a[8:11], v[124:127], v[68:83]\n"
    "ds_read_b128 v[120:123], v20 offset:39936\n"
    "buffer_load_dwordx4 v[152:155], v14, s[16:19], s91 offen offset:2048 sc1\n"
    "v_mul_f32 v105, 0xbdd2d3e8, v6\n"
    "v_mul_f32 v40, v52, v52\n"
    "v_mul_f32 v41, v53, v53\n"
    "v_fma_f32 v40, v40, v101, v100\n"
    "v_mfma_f32_32x32x16_bf16 v[84:99], a[136:139], v[124:127], v[84:99]\n"
    "s_waitcnt lgkmcnt(4)\n"
    "v_mul_f32 v42, v54, v54\n"
    "v_fma_f32 v41, v41, v101, v100\n"
    "v_mul_f32 v43, v55, v55\n"
    "v_mul_f32 v40, v40, v52\n"
    "v_fma_f32 v42, v42, v101, v100\n"
    "v_mul_f32 v44, v56, v56\n"
    "v_mfma_f32_32x32x16_bf16 v[68:83], a[12:15], v[128:131], v[68:83]\n"
    "ds_read_b128 v[124:127], v20 offset:40960\n"
    "v_mul_f32 v41, v41, v53\n"
    "v_fma_f32 v43, v43, v101, v100\n"
    "v_mul_f32 v45, v57, v57\n"
    "v_mul_f32 v42, v42, v54\n"
    "v_mfma_f32_32x32x16_bf16 v[84:99], a[140:143], v[128:131], v[84:99]\n"
    "s_waitcnt lgkmcnt(4)\n"
    "v_exp_f32 v40, v40\n"
    "v_fma_f32 v44, v44, v101, v100\n"
    "v_mul_f32 v46, v58, v58\n"
    "v_mul_f32 v43, v43, v55\n"
    "v_mfma_f32_32x32x16_bf16 v[68:83], a[16:19], v[132:135], v[68:83]\n"
    "ds_read_b128 v[128:131], v20 offset:41984\n"
    "v_exp_f32 v41, v41\n"
    "v_fma_f32 v45, v45, v101, v100\n"
    "v_mul_f32 v47, v59, v59\n"
    "v_fma_f32 v40, v40, v102, v102\n"
    "v_mfma_f32_32x32x16_bf16 v[84:99], a[144:147], v[132:135], v[84:99]\n"
    "s_waitcnt lgkmcnt(4)\n"
    "buffer_load_dwordx4 v[156:159], v14, s[16:19], s91 offen offset:3072 sc1\n"
    "v_mul_f32 v44, v44, v56\n"
    "v_exp_f32 v42, v42\n"
    "v_fma_f32 v46, v46, v101, v100\n"
    "v_fma_f32 v41, v41, v102, v102\n"
    "v_mfma_f32_32x32x16_bf16 v[68:83], a[20:23], v[24:27], v[68:83]\n"
    "ds_read_b128 v[132:135], v20 offset:43008\n"
    "v_mul_f32 v45, v45, v57\n"
    "v_exp_f32 v43, v43\n"
    "v_fma_f32 v47, v47, v101, v100\n"
    "v_mfma_f32_32x32x16_bf16 v[84:99], a[148:151], v[24:27], v[84:99]\n"
    "s_waitcnt lgkmcnt(4)\n"
    "v_rcp_f32 v40, v40\n"
    "v_fma_f32 v42, v42, v102, v102\n"
    "v_mul_f32 v46, v46, v58\n"
    "v_exp_f32 v44, v44\n"
    "v_mfma_f32_32x32x16_bf16 v[68:83], a[24:27], v[116:119], v[68:83]\n"
    "ds_read_b128 v[24:27], v20 offset:44032\n"
    "v_rcp_f32 v41, v41\n"
    "v_fma_f32 v43, v43, v102, v102\n"
    "v_mul_f32 v47, v47, v59\n"
    "v_mfma_f32_32x32x16_bf16 v[84:99], a[152:155], v[116:119], v[84:99]\n"
    "s_waitcnt lgkmcnt(4)\n"
    "v_exp_f32 v45, v45\n"
    "v_mul_f32 v40, v40, v52\n"
    "v_rcp_f32 v42, v42\n"
    "v_fma_f32 v44, v44, v102, v102\n"
    "v_mfma_f32_32x32x16_bf16 v[68:83], a[28:31], v[120:123], v[68:83]\n"
    "ds_read_b128 v[116:119], v20 offset:45056\n"
    "s_add_u32 s91, s91, 0x1000\n"
    "buffer_load_dwordx4 v[160:163], v14, s[16:19], s91 offen sc1\n"
    "v_exp_f32 v46, v46\n"
    "v_mul_f32 v41, v41, v53\n"
    "v_mfma_f32_32x32x16_bf16 v[84:99], a[156:159], v[120:123], v[84:99]\n"
    "s_waitcnt lgkmcnt(4)\n"
    "v_rcp_f32 v43, v43\n"
    "v_fma_f32 v45, v45, v102, v102\n"
    "v_exp_f32 v47, v47\n"
    "v_mul_f32 v42, v42, v54\n"
    "v_mfma_f32_32x32x16_bf16 v[68:83], a[32:35], v[124:127], v[68:83]\n"
    "ds_read_b128 v[120:123], v20 offset:46080\n"
    "v_rcp_f32 v44, v44\n"
    "v_fma_f32 v46, v46, v102, v102\n"
    "v_cvt_pk_bf16_f32 v52, v40, v41\n"
    "v_mul_f32 v43, v43, v55\n"
    "v_mfma_f32_32x32x16_bf16 v[84:99], a[160:163], v[124:127], v[84:99]\n"
    "s_waitcnt lgkmcnt(4)\n"
    "v_rcp_f32 v45, v45\n"
    "v_fma_f32 v47, v47, v102, v102\n"
    "v_mul_f32 v44, v44, v56\n"
    "v_rcp_f32 v46, v46\n"
    "v_mfma_f32_32x32x16_bf16 v[68:83], a[36:39], v[128:131], v[68:83]\n"
    "ds_read_b128 v[124:127], v20 offset:47104\n"
    "buffer_load_dwordx4 v[164:167], v14, s[16:19], s91 offen offset:1024 sc1\n"
    "v_cvt_pk_bf16_f32 v53, v42, v43\n"
    "v_mul_f32 v45, v45, v57\n"
    "v_rcp_f32 v47, v47\n"
    "v_mfma_f32_32x32x16_bf16 v[84:99], a[164:167], v[128:131], v[84:99]\n"
    "s_waitcnt lgkmcnt(4)\n"
    "v_mul_f32 v46, v46, v58\n"
    "v_cvt_pk_bf16_f32 v54, v44, v45\n"
    "v_mul_f32 v47, v47, v59\n"
    "v_cvt_pk_bf16_f32 v55, v46, v47\n"
    "v_mul_f32 v40, v60, v60\n"
    "v_mul_f32 v41, v61, v61\n"
    "v_mfma_f32_32x32x16_bf16 v[68:83], a[40:43], v[132:135], v[68:83]\n"
    "ds_read_b128 v[128:131], v20 offset:48128\n"
    "buffer_store_dwordx4 v[52:55], v14, s[16:19], s84 offen sc1\n"
    "v_fma_f32 v40, v40, v101, v100\n"
    "v_mul_f32 v42, v62, v62\n"
    "v_fma_f32 v41, v41, v101, v100\n"
    "v_mfma_f32_32x32x16_bf16 v[84:99], a[168:171], v[132:135], v[84:99]\n"
    "s_waitcnt lgkmcnt(4)\n"
    "v_mul_f32 v43, v63, v63\n"
    "v_mul_f32 v40, v40, v60\n"
    "v_fma_f32 v42, v42, v101, v100\n"
    "v_mul_f32 v44, v64, v64\n"
    "v_mul_f32 v41, v41, v61\n"
    "v_fma_f32 v43, v43, v101, v100\n"
    "v_mfma_f32_32x32x16_bf16 v[68:83], a[44:47], v[24:27], v[68:83]\n"
    "ds_read_b128 v[132:135], v22 offset:32768\n"
    "v_mul_f32 v45, v65, v65\n"
    "v_mul_f32 v42, v42, v62\n"
    "v_exp_f32 v40, v40\n"
    "v_fma_f32 v44, v44, v101, v100\n"
    "v_mfma_f32_32x32x16_bf16 v[84:99], a[172:175], v[24:27], v[84:99]\n"
    "s_waitcnt lgkmcnt(4)\n"
    "buffer_load_dwordx4 v[168:171], v14, s[16:19], s91 offen offset:2048 sc1\n"
    "v_mul_f32 v46, v66, v66\n"
    "v_mul_f32 v43, v43, v63\n"
    "v_exp_f32 v41, v41\n"
    "v_fma_f32 v45, v45, v101, v100\n"
    "v_mfma_f32_32x32x16_bf16 v[68:83], a[48:51], v[116:119], v[68:83]\n"
    "ds_read_b128 v[24:27], v22 offset:33792\n"
    "v_mul_f32 v47, v67, v67\n"
    "v_fma_f32 v40, v40, v102, v102\n"
    "v_mul_f32 v44, v44, v64\n"
    "v_exp_f32 v42, v42\n"
    "v_mfma_f32_32x32x16_bf16 v[84:99], a[176:179], v[116:119], v[84:99]\n"
    "s_waitcnt lgkmcnt(4)\n"
    "v_fma_f32 v46, v46, v101, v100\n"
    "v_fma_f32 v41, v41, v102, v102\n"
    "v_mul_f32 v45, v45, v65\n"
    "v_exp_f32 v43, v43\n"
    "v_fma_f32 v47, v47, v101, v100\n"
    "v_mfma_f32_32x32x16_bf16 v[68:83], a[52:55], v[120:123], v[68:83]\n"
    "ds_read_b128 v[116:119], v22 offset:34816\n"
    "v_rcp_f32 v40, v40\n"
    "v_fma_f32 v42, v42, v102, v102\n"
    "v_mul_f32 v46, v46, v66\n"
    "v_mfma_f32_32x32x16_bf16 v[84:99], a[180:183], v[120:123], v[84:99]\n"
    "s_waitcnt lgkmcnt(4)\n"
    "v_exp_f32 v44, v44\n"
    "v_rcp_f32 v41, v41\n"
    "v_fma_f32 v43, v43, v102, v102\n"
    "v_mul_f32 v47, v47, v67\n"
    "v_mfma_f32_32x32x16_bf16 v[68:83], a[56:59], v[124:127], v[68:83]\n"
    "ds_read_b128 v[120:123], v22 offset:35840\n"
    "buffer_load_dwordx4 v[172:175], v14, s[16:19], s91 offen offset:3072 sc1\n"
    "v_exp_f32 v45, v45\n"
    "v_mul_f32 v40, v40, v60\n"
    "v_mfma_f32_32x32x16_bf16 v[84:99], a[184:187], v[124:127], v[84:99]\n"
    "s_waitcnt lgkmcnt(4)\n"
    "v_rcp_f32 v42, v42\n"
    "v_fma_f32 v44, v44, v102, v102\n"
    "v_exp_f32 v46, v46\n"
    "v_mul_f32 v41, v41, v61\n"
    "v_mfma_f32_32x32x16_bf16 v[68:83], a[60:63], v[128:131], v[68:83]\n"
    "ds_read_b128 v[124:127], v22 offset:36864\n"
    "v_rcp_f32 v43, v43\n"
    "v_fma_f32 v45, v45, v102, v102\n"
    "v_exp_f32 v47, v47\n"
    "v_mfma_f32_32x32x16_bf16 v[84:99], a[188:191], v[128:131], v[84:99]\n"
    "s_waitcnt lgkmcnt(4)\n"
    "v_mul_f32 v42, v42, v62\n"
    "v_rcp_f32 v44, v44\n"
    "v_fma_f32 v46, v46, v102, v102\n"
    "v_cvt_pk_bf16_f32 v60, v40, v41\n"
    "v_mul_f32 v43, v43, v63\n"
    "v_mfma_f32_32x32x16_bf16 v[84:99], v[34:37], v[48:51], v[84:99]\n"
    "v_rcp_f32 v45, v45\n"
    "v_fma_f32 v47, v47, v102, v102\n"
    "v_mul_f32 v44, v44, v64\n"
    "v_rcp_f32 v46, v46\n"
    "v_mfma_f32_32x32x16_bf16 v[68:83], a[64:67], v[132:135], v[68:83]\n"
    "s_waitcnt lgkmcnt(3)\n"
    "ds_read_b128 v[128:131], v22 offset:37888\n"
    "v_cvt_pk_bf16_f32 v61, v42, v43\n"
    "v_mul_f32 v45, v45, v65\n"
    "v_rcp_f32 v47, v47\n"
    "v_mul_f32 v46, v46, v66\n"
    "v_mfma_f32_32x32x16_bf16 v[68:83], a[68:71], v[24:27], v[68:83]\n"
    "s_waitcnt lgkmcnt(3)\n"
    "ds_read_b128 v[132:135], v22 offset:38912\n"
    "v_cvt_pk_bf16_f32 v62, v44, v45\n"
    "v_mul_f32 v47, v47, v67\n"
    "v_cvt_pk_bf16_f32 v63, v46, v47\n"
    "v_mfma_f32_32x32x16_bf16 v[68:83], a[72:75], v[116:119], v[68:83]\n"
    "s_waitcnt lgkmcnt(3)\n"
    "ds_read_b128 v[24:27], v22 offset:39936\n"
    "ds_write_b128 v23, v[92:95] offset:8192\n"
    "v_mfma_f32_32x32x16_bf16 v[68:83], a[76:79], v[120:123], v[68:83]\n"
    "s_waitcnt lgkmcnt(4)\n"
    "ds_read_b128 v[116:119], v22 offset:40960\n"
    "ds_write_b128 v23, v[96:99] offset:9216\n"
    "buffer_store_dwordx4 v[60:63], v14, s[16:19], s84 offen offset:1024 sc1\n"
    "s_add_u32 s84, s84, 0x16000\n"
    "v_mfma_f32_32x32x16_bf16 v[68:83], a[80:83], v[124:127], v[68:83]\n"
    "s_waitcnt lgkmcnt(5)\n"
    "ds_read_b128 v[120:123], v22 offset:41984\n"
    "s_waitcnt lgkmcnt(1)\n"
    "s_barrier\n"
    "v_mfma_f32_32x32x16_bf16 v[68:83], a[84:87], v[128:131], v[68:83]\n"
    "ds_read_b128 v[124:127], v22 offset:43008\n"
    "ds_read_b128 v[108:111], v29 offset:8192\n"
    "ds_read_b128 v[112:115], v29 offset:9216\n"
    "v_mfma_f32_32x32x16_bf16 v[68:83], a[88:91], v[132:135], v[68:83]\n"
    "ds_read_b128 v[128:131], v22 offset:44032\n"
    "v_mfma_f32_32x32x16_bf16 v[68:83], a[92:95], v[24:27], v[68:83]\n"
    "ds_read_b128 v[132:135], v22 offset:45056\n"
    "v_mfma_f32_32x32x16_bf16 v[68:83], a[96:99], v[116:119], v[68:83]\n"
    "s_waitcnt lgkmcnt(5)\n"
    "ds_read_b128 v[24:27], v22 offset:46080\n"
    "v_mfma_f32_32x32x16_bf16 v[68:83], a[100:103], v[120:123], v[68:83]\n"
    "s_waitcnt lgkmcnt(5)\n"
    "ds_read_b128 v[116:119], v22 offset:47104\n"
    "s_add_u32 s91, s91, 0x1000\n"
    "buffer_load_dwordx4 v[176:179], v17, s[16:19], s91 offen sc1\n"
    "s_waitcnt lgkmcnt(5)\n"
    "v_add_f32 v108, v108, v84\n"
    "v_add_f32 v109, v109, v85\n"
    "v_add_f32 v110, v110, v86\n"
    "v_add_f32 v111, v111, v87\n"
    "v_mfma_f32_32x32x16_bf16 v[68:83], a[104:107], v[124:127], v[68:83]\n"
    "s_waitcnt lgkmcnt(3)\n"
    "ds_read_b128 v[120:123], v22 offset:48128\n"
    "v_add_f32 v112, v112, v88\n"
    "v_add_f32 v113, v113, v89\n"
    "v_add_f32 v114, v114, v90\n"
    "v_add_f32 v115, v115, v91\n"
    "v_mul_f32 v40, v108, v108\n"
    "v_mfma_f32_32x32x16_bf16 v[68:83], a[108:111], v[128:131], v[68:83]\n"
    "s_waitcnt lgkmcnt(3)\n"
    "buffer_load_dwordx4 v[180:183], v17, s[16:19], s91 offen offset:1024 sc1\n"
    "v_mul_f32 v41, v109, v109\n"
    "v_fma_f32 v40, v40, v105, v104\n"
    "v_mul_f32 v42, v110, v110\n"
    "v_fma_f32 v41, v41, v105, v104\n"
    "v_mul_f32 v43, v111, v111\n"
    "v_mfma_f32_32x32x16_bf16 v[68:83], a[112:115], v[132:135], v[68:83]\n"
    "s_waitcnt lgkmcnt(2)\n"
    "v_mul_f32 v40, v40, v108\n"
    "v_fma_f32 v42, v42, v105, v104\n"
    "v_mul_f32 v44, v112, v112\n"
    "v_mul_f32 v41, v41, v109\n"
    "v_fma_f32 v43, v43, v105, v104\n"
    "v_mul_f32 v45, v113, v113\n"
    "v_mfma_f32_32x32x16_bf16 v[68:83], a[116:119], v[24:27], v[68:83]\n"
    "s_waitcnt lgkmcnt(1)\n"
    "buffer_load_dwordx4 v[184:187], v17, s[16:19], s91 offen offset:2048 sc1\n"
    "v_mul_f32 v42, v42, v110\n"
    "v_exp_f32 v40, v40\n"
    "v_fma_f32 v44, v44, v105, v104\n"
    "v_mul_f32 v46, v114, v114\n"
    "v_mfma_f32_32x32x16_bf16 v[68:83], a[120:123], v[116:119], v[68:83]\n"
    "s_waitcnt lgkmcnt(0)\n"
    "v_mul_f32 v43, v43, v111\n"
    "v_exp_f32 v41, v41\n"
    "v_fma_f32 v45, v45, v105, v104\n"
    "v_mul_f32 v47, v115, v115\n"
    "v_fma_f32 v40, v40, v106, v106\n"
    "v_mfma_f32_32x32x16_bf16 v[68:83], a[124:127], v[120:123], v[68:83]\n"
    "buffer_load_dwordx4 v[188:191], v17, s[16:19], s91 offen offset:3072 sc1\n"
    "v_mul_f32 v44, v44, v112\n"
    "v_exp_f32 v42, v42\n"
    "v_fma_f32 v46, v46, v105, v104\n"
    "v_fma_f32 v41, v41, v106, v106\n"
    "s_waitcnt vmcnt(4)\n"
    "s_barrier\n"
    "buffer_store_dwordx2 v[12:13], v18, s[16:19], s77 offen sc1\n"
    "s_add_u32 s77, s77, 0x20000\n"
    "v_mfma_f32_32x32x16_bf16 v[68:83], v[30:33], v[48:51], v[68:83]\n"
    "v_mul_f32 v45, v45, v113\n"
    "v_exp_f32 v43, v43\n"
    "v_fma_f32 v47, v47, v105, v104\n"
    "v_rcp_f32 v40, v40\n"
    "v_lshlrev_b32 v2, 4, v8\n"
    "s_lshl_b32 s52, s51, 7\n"
    "s_lshl3_add_u32 s54, s50, s49\n"
    "s_mul_i32 s54, s54, 0x5800\n"
    "s_add_u32 s54, s54, 0x962000\n"
    "buffer_load_dwordx4 v[48:51], v2, s[40:43], s52 offen\n"
    "v_lshl_or_b32 v7, v9, 10, v2\n"
    "v_fma_f32 v42, v42, v106, v106\n"
    "v_mul_f32 v46, v46, v114\n"
    "v_exp_f32 v44, v44\n"
    "v_rcp_f32 v41, v41\n"
    "v_fma_f32 v43, v43, v106, v106\n"
    "v_mul_f32 v47, v47, v115\n"
    "v_exp_f32 v45, v45\n"
    "v_mul_f32 v40, v40, v108\n"
    "v_rcp_f32 v42, v42\n"
    "v_fma_f32 v44, v44, v106, v106\n"
    "v_exp_f32 v46, v46\n"
    "v_mul_f32 v41, v41, v109\n"
    "v_rcp_f32 v43, v43\n"
    "v_fma_f32 v45, v45, v106, v106\n"
    "v_exp_f32 v47, v47\n"
    "v_mul_f32 v42, v42, v110\n"
    "v_rcp_f32 v44, v44\n"
    "v_fma_f32 v46, v46, v106, v106\n"
    "v_cvt_pk_bf16_f32 v108, v40, v41\n"
    "v_mul_f32 v43, v43, v111\n"
    "v_rcp_f32 v45, v45\n"
    "v_fma_f32 v47, v47, v106, v106\n"
    "v_mul_f32 v44, v44, v112\n"
    "v_rcp_f32 v46, v46\n"
    "v_cvt_pk_bf16_f32 v109, v42, v43\n"
    "v_mul_f32 v45, v45, v113\n"
    "v_rcp_f32 v47, v47\n"
    "v_mul_f32 v46, v46, v114\n"
    "v_cvt_pk_bf16_f32 v110, v44, v45\n"
    "v_mul_f32 v47, v47, v115\n"
    "v_cvt_pk_bf16_f32 v111, v46, v47\n"
    "buffer_store_dwordx4 v[108:111], v16, s[16:19], s85 offen sc1\n"
    "v_pk_mul_f32 v[40:41], v[68:69], v[68:69]\n"
    "v_pk_mul_f32 v[42:43], v[70:71], v[70:71]\n"
    "v_pk_mul_f32 v[44:45], v[72:73], v[72:73]\n"
    "v_pk_mul_f32 v[46:47], v[74:75], v[74:75]\n"
    "v_pk_fma_f32 v[40:41], v[40:41], v[104:105], v[104:105] op_sel:[0,1,0] op_sel_hi:[1,1,0]\n"
    "v_pk_fma_f32 v[42:43], v[42:43], v[104:105], v[104:105] op_sel:[0,1,0] op_sel_hi:[1,1,0]\n"
    "v_pk_fma_f32 v[44:45], v[44:45], v[104:105], v[104:105] op_sel:[0,1,0] op_sel_hi:[1,1,0]\n"
    "v_pk_fma_f32 v[46:47], v[46:47], v[104:105], v[104:105] op_sel:[0,1,0] op_sel_hi:[1,1,0]\n"
    "v_pk_mul_f32 v[40:41], v[40:41], v[68:69]\n"
    "v_pk_mul_f32 v[42:43], v[42:43], v[70:71]\n"
    "v_pk_mul_f32 v[44:45], v[44:45], v[72:73]\n"
    "v_pk_mul_f32 v[46:47], v[46:47], v[74:75]\n"
    "v_exp_f32 v40, v40\n"
    "s_add_u32 s78, s78, 0x20000\n"
    "s_not_b64 s[60:61], s[56:57]\n"
    "s_not_b64 s[62:63], s[56:57]\n"
    "s_buffer_load_dwordx4 s[60:63], s[16:19], s78 glc\n"
    "v_exp_f32 v41, v41\n"
    "v_exp_f32 v42, v42\n"
    "v_exp_f32 v43, v43\n"
    "v_exp_f32 v44, v44\n"
    "v_exp_f32 v45, v45\n"
    "v_exp_f32 v46, v46\n"
    "v_exp_f32 v47, v47\n"
    "v_pk_fma_f32 v[40:41], v[40:41], v[106:107], v[106:107] op_sel:[0,0,0] op_sel_hi:[1,0,0]\n"
    "v_pk_fma_f32 v[42:43], v[42:43], v[106:107], v[106:107] op_sel:[0,0,0] op_sel_hi:[1,0,0]\n"
    "v_pk_fma_f32 v[44:45], v[44:45], v[106:107], v[106:107] op_sel:[0,0,0] op_sel_hi:[1,0,0]\n"
    "v_pk_fma_f32 v[46:47], v[46:47], v[106:107], v[106:107] op_sel:[0,0,0] op_sel_hi:[1,0,0]\n"
    "v_rcp_f32 v40, v40\n"
    "v_rcp_f32 v41, v41\n"
    "v_rcp_f32 v42, v42\n"
    "v_rcp_f32 v43, v43\n"
    "v_rcp_f32 v44, v44\n"
    "v_rcp_f32 v45, v45\n"
    "v_rcp_f32 v46, v46\n"
    "v_rcp_f32 v47, v47\n"
    "v_pk_mul_f32 v[40:41], v[40:41], v[68:69]\n"
    "v_pk_mul_f32 v[42:43], v[42:43], v[70:71]\n"
    "v_pk_mul_f32 v[44:45], v[44:45], v[72:73]\n"
    "v_pk_mul_f32 v[46:47], v[46:47], v[74:75]\n"
    "v_cvt_pk_bf16_f32 v68, v40, v41\n"
    "v_cvt_pk_bf16_f32 v69, v42, v43\n"
    "v_cvt_pk_bf16_f32 v70, v44, v45\n"
    "v_cvt_pk_bf16_f32 v71, v46, v47\n"
    "buffer_store_dwordx4 v[68:71], v14, s[16:19], s84 offen sc1\n"
    "v_pk_mul_f32 v[40:41], v[76:77], v[76:77]\n"
    "v_pk_mul_f32 v[42:43], v[78:79], v[78:79]\n"
    "v_pk_mul_f32 v[44:45], v[80:81], v[80:81]\n"
    "v_pk_mul_f32 v[46:47], v[82:83], v[82:83]\n"
    "v_pk_fma_f32 v[40:41], v[40:41], v[104:105], v[104:105] op_sel:[0,1,0] op_sel_hi:[1,1,0]\n"
    "v_pk_fma_f32 v[42:43], v[42:43], v[104:105], v[104:105] op_sel:[0,1,0] op_sel_hi:[1,1,0]\n"
    "v_pk_fma_f32 v[44:45], v[44:45], v[104:105], v[104:105] op_sel:[0,1,0] op_sel_hi:[1,1,0]\n"
    "v_pk_fma_f32 v[46:47], v[46:47], v[104:105], v[104:105] op_sel:[0,1,0] op_sel_hi:[1,1,0]\n"
    "v_pk_mul_f32 v[40:41], v[40:41], v[76:77]\n"
    "v_pk_mul_f32 v[42:43], v[42:43], v[78:79]\n"
    "v_pk_mul_f32 v[44:45], v[44:45], v[80:81]\n"
    "v_pk_mul_f32 v[46:47], v[46:47], v[82:83]\n"
    "v_exp_f32 v40, v40\n"
    "v_exp_f32 v41, v41\n"
    "v_exp_f32 v42, v42\n"
    "v_exp_f32 v43, v43\n"
    "v_exp_f32 v44, v44\n"
    "v_exp_f32 v45, v45\n"
    "v_exp_f32 v46, v46\n"
    "v_exp_f32 v47, v47\n"
    "v_pk_fma_f32 v[40:41], v[40:41], v[106:107], v[106:107] op_sel:[0,0,0] op_sel_hi:[1,0,0]\n"
    "v_pk_fma_f32 v[42:43], v[42:43], v[106:107], v[106:107] op_sel:[0,0,0] op_sel_hi:[1,0,0]\n"
    "v_pk_fma_f32 v[44:45], v[44:45], v[106:107], v[106:107] op_sel:[0,0,0] op_sel_hi:[1,0,0]\n"
    "v_pk_fma_f32 v[46:47], v[46:47], v[106:107], v[106:107] op_sel:[0,0,0] op_sel_hi:[1,0,0]\n"
    "v_rcp_f32 v40, v40\n"
    "v_rcp_f32 v41, v41\n"
    "v_rcp_f32 v42, v42\n"
    "v_rcp_f32 v43, v43\n"
    "v_rcp_f32 v44, v44\n"
    "v_rcp_f32 v45, v45\n"
    "v_rcp_f32 v46, v46\n"
    "v_rcp_f32 v47, v47\n"
    "v_pk_mul_f32 v[40:41], v[40:41], v[76:77]\n"
    "v_pk_mul_f32 v[42:43], v[42:43], v[78:79]\n"
    "v_pk_mul_f32 v[44:45], v[44:45], v[80:81]\n"
    "v_pk_mul_f32 v[46:47], v[46:47], v[82:83]\n"
    "v_cvt_pk_bf16_f32 v76, v40, v41\n"
    "v_cvt_pk_bf16_f32 v77, v42, v43\n"
    "v_cvt_pk_bf16_f32 v78, v44, v45\n"
    "v_cvt_pk_bf16_f32 v79, v46, v47\n"
    "buffer_store_dwordx4 v[76:79], v14, s[16:19], s84 offen offset:1024 sc1\n"
    "s_xor_b64 s[92:93], s[60:61], s[56:57]\n"
    "s_xor_b64 vcc, s[62:63], s[56:57]\n"
    "s_or_b64 s[92:93], s[92:93], vcc\n"
    "s_cbranch_scc0 .Lhh_s7d\n"
    "s_mov_b32 s58, 48\n"
    ".Lhh_s7a:\n"
    "s_buffer_load_dwordx4 s[60:63], s[16:19], s78 glc\n"
    "s_sleep 1\n"
    "s_xor_b64 s[92:93], s[60:61], s[56:57]\n"
    "s_xor_b64 vcc, s[62:63], s[56:57]\n"
    "s_or_b64 s[92:93], s[92:93], vcc\n"
    "s_cbranch_scc0 .Lhh_s7d\n"
    "s_sleep 1\n"
    "s_xor_b64 s[92:93], s[60:61], s[56:57]\n"
    "s_xor_b64 vcc, s[62:63], s[56:57]\n"
    "s_or_b64 s[92:93], s[92:93], vcc\n"
    "s_cbranch_scc0 .Lhh_s7d\n"
    "s_sleep 1\n"
    "s_xor_b64 s[92:93], s[60:61], s[56:57]\n"
    "s_xor_b64 vcc, s[62:63], s[56:57]\n"
    "s_or_b64 s[92:93], s[92:93], vcc\n"
    "s_cbranch_scc0 .Lhh_s7d\n"
    "s_sub_u32 s58, s58, 1\n"
    "s_cbranch_scc0 .Lhh_s7a\n"
    "s_branch .Lhh_s7\n"
    ".Lhh_s7d:\n"
    "s_add_u32 s82, s54, 0\n"
    "buffer_load_dwordx4 a[0:3], v14, s[16:19], s82 offen sc1\n"
    "buffer_load_dwordx4 a[4:7], v14, s[16:19], s82 offen offset:1024 sc1\n"
    "buffer_load_dwordx4 a[8:11], v14, s[16:19], s82 offen offset:2048 sc1\n"
    "buffer_load_dwordx4 a[12:15], v14, s[16:19], s82 offen offset:3072 sc1\n"
    "s_add_u32 s82, s54, 0x1000\n"
    "buffer_load_dwordx4 a[16:19], v14, s[16:19], s82 offen sc1\n"
    "buffer_load_dwordx4 a[20:23], v14, s[16:19], s82 offen offset:1024 sc1\n"
    "buffer_load_dwordx4 a[24:27], v14, s[16:19], s82 offen offset:2048 sc1\n"
    "buffer_load_dwordx4 a[28:31], v14, s[16:19], s82 offen offset:3072 sc1\n"
    "s_add_u32 s82, s54, 0x2000\n"
    "buffer_load_dwordx4 a[32:35], v14, s[16:19], s82 offen sc1\n"
    "buffer_load_dwordx4 a[36:39], v14, s[16:19], s82 offen offset:1024 sc1\n"
    "v_lshlrev_b32 v120, 16, v136\n"
    "v_and_b32 v121, 0xffff0000, v136\n"
    "v_lshlrev_b32 v122, 16, v137\n"
    "v_and_b32 v123, 0xffff0000, v137\n"
    "v_lshlrev_b32 v124, 16, v138\n"
    "v_and_b32 v125, 0xffff0000, v138\n"
    "v_lshlrev_b32 v126, 16, v139\n"
    "v_and_b32 v127, 0xffff0000, v139\n"
    "v_lshlrev_b32 v128, 16, v140\n"
    "v_and_b32 v129, 0xffff0000, v140\n"
    "v_lshlrev_b32 v130, 16, v141\n"
    "v_and_b32 v131, 0xffff0000, v141\n"
    "v_lshlrev_b32 v132, 16, v142\n"
    "v_and_b32 v133, 0xffff0000, v142\n"
    "v_lshlrev_b32 v134, 16, v143\n"
    "v_and_b32 v135, 0xffff0000, v143\n"
    "s_waitcnt vmcnt(13)\n"
    "v_lshlrev_b32 v2, 16, v48\n"
    "v_and_b32 v3, 0xffff0000, v48\n"
    "v_add_f32 v120, v120, v2\n"
    "v_add_f32 v121, v121, v3\n"
    "v_add_f32 v128, v128, v2\n"
    "v_add_f32 v129, v129, v3\n"
    "v_lshlrev_b32 v2, 16, v49\n"
    "v_and_b32 v3, 0xffff0000, v49\n"
    "v_add_f32 v122, v122, v2\n"
    "v_add_f32 v123, v123, v3\n"
    "v_add_f32 v130, v130, v2\n"
    "v_add_f32 v131, v131, v3\n"
    "v_lshlrev_b32 v2, 16, v50\n"
    "v_and_b32 v3, 0xffff0000, v50\n"
    "v_add_f32 v124, v124, v2\n"
    "v_add_f32 v125, v125, v3\n"
    "v_add_f32 v132, v132, v2\n"
    "v_add_f32 v133, v133, v3\n"
    "v_lshlrev_b32 v2, 16, v51\n"
    "v_and_b32 v3, 0xffff0000, v51\n"
    "v_add_f32 v126, v126, v2\n"
    "v_add_f32 v127, v127, v3\n"
    "v_add_f32 v134, v134, v2\n"
    "v_add_f32 v135, v135, v3\n"
    "s_lshl3_add_u32 s53, s50, s49\n"
    "s_lshl_b32 s53, s53, 13\n"
    "s_lshl_b32 s55, s51, 7\n"
    "s_add_u32 s53, s53, s55\n"
    "s_add_u32 s55, s53, 0x8000\n"
    "s_waitcnt vmcnt(10)\n"
    "s_barrier\n"
    "buffer_store_dwordx2 v[12:13], v18, s[16:19], s77 offen sc1\n"
    "s_waitcnt vmcnt(10)\n"
    "v_mfma_f32_32x32x16_bf16 v[52:67], v[192:195], a[0:3], 0\n"
    "buffer_load_dwordx4 a[40:43], v14, s[16:19], s82 offen offset:2048 sc1\n"
    "v_mfma_f32_32x32x16_bf16 v[68:83], v[196:199], a[0:3], 0\n"
    "s_waitcnt vmcnt(10)\n"
    "v_mfma_f32_32x32x16_bf16 v[52:67], v[200:203], a[4:7], v[52:67]\n"
    "buffer_load_dwordx4 a[44:47], v14, s[16:19], s82 offen offset:3072 sc1\n"
    "v_mfma_f32_32x32x16_bf16 v[68:83], v[204:207], a[4:7], v[68:83]\n"
    "s_waitcnt vmcnt(10)\n"
    "v_mfma_f32_32x32x16_bf16 v[52:67], v[208:211], a[8:11], v[52:67]\n"
    "s_add_u32 s82, s54, 0x3000\n"
    "buffer_load_dwordx4 a[48:51], v14, s[16:19], s82 offen sc1\n"
    "v_mfma_f32_32x32x16_bf16 v[68:83], v[212:215], a[8:11], v[68:83]\n"
    "s_waitcnt vmcnt(10)\n"
    "v_mfma_f32_32x32x16_bf16 v[52:67], v[216:219], a[12:15], v[52:67]\n"
    "buffer_load_dwordx4 a[52:55], v14, s[16:19], s82 offen offset:1024 sc1\n"
    "v_mfma_f32_32x32x16_bf16 v[68:83], v[220:223], a[12:15], v[68:83]\n"
    "s_waitcnt vmcnt(10)\n"
    "v_mfma_f32_32x32x16_bf16 v[52:67], v[224:227], a[16:19], v[52:67]\n"
    "buffer_load_dwordx4 a[56:59], v14, s[16:19], s82 offen offset:2048 sc1\n"
    "v_mfma_f32_32x32x16_bf16 v[68:83], v[228:231], a[16:19], v[68:83]\n"
    "s_waitcnt vmcnt(10)\n"
    "v_mfma_f32_32x32x16_bf16 v[52:67], v[232:235], a[20:23], v[52:67]\n"
    "buffer_load_dwordx4 a[60:63], v14, s[16:19], s82 offen offset:3072 sc1\n"
    "v_mfma_f32_32x32x16_bf16 v[68:83], v[236:239], a[20:23], v[68:83]\n"
    "s_waitcnt lgkmcnt(0)\n"
    "s_add_u32 s78, s78, 0x20000\n"
    "s_not_b64 s[60:61], s[56:57]\n"
    "s_not_b64 s[62:63], s[56:57]\n"
    "s_buffer_load_dwordx4 s[60:63], s[16:19], s78 glc\n"
    "s_waitcnt vmcnt(10)\n"
    "v_mfma_f32_32x32x16_bf16 v[52:67], v[240:243], a[24:27], v[52:67]\n"
    "s_add_u32 s82, s54, 0x4000\n"
    "buffer_load_dwordx4 a[64:67], v14, s[16:19], s82 offen sc1\n"
    "v_mfma_f32_32x32x16_bf16 v[68:83], v[244:247], a[24:27], v[68:83]\n"
    "s_waitcnt vmcnt(10)\n"
    "v_mfma_f32_32x32x16_bf16 v[52:67], v[248:251], a[28:31], v[52:67]\n"
    "buffer_load_dwordx4 a[68:71], v14, s[16:19], s82 offen offset:1024 sc1\n"
    "v_mfma_f32_32x32x16_bf16 v[68:83], v[252:255], a[28:31], v[68:83]\n"
    "s_waitcnt vmcnt(10)\n"
    "v_mfma_f32_32x32x16_bf16 v[52:67], a[192:195], a[32:35], v[52:67]\n"
    "buffer_load_dwordx4 a[72:75], v14, s[16:19], s82 offen offset:2048 sc1\n"
    "v_mfma_f32_32x32x16_bf16 v[68:83], a[196:199], a[32:35], v[68:83]\n"
    "s_waitcnt vmcnt(10)\n"
    "v_mfma_f32_32x32x16_bf16 v[52:67], a[200:203], a[36:39], v[52:67]\n"
    "buffer_load_dwordx4 a[76:79], v14, s[16:19], s82 offen offset:3072 sc1\n"
    "v_mfma_f32_32x32x16_bf16 v[68:83], a[204:207], a[36:39], v[68:83]\n"
    "s_waitcnt vmcnt(9)\n"
    "v_mfma_f32_32x32x16_bf16 v[52:67], a[208:211], a[40:43], v[52:67]\n"
    "s_add_u32 s82, s54, 0x5000\n"
    "buffer_load_dwordx4 a[80:83], v17, s[16:19], s82 offen sc1\n"
    "v_mfma_f32_32x32x16_bf16 v[68:83], a[212:215], a[40:43], v[68:83]\n"
    "s_waitcnt vmcnt(9)\n"
    "v_mfma_f32_32x32x16_bf16 v[52:67], a[216:219], a[44:47], v[52:67]\n"
    "buffer_load_dwordx4 a[84:87], v17, s[16:19], s82 offen offset:1024 sc1\n"
    "v_mfma_f32_32x32x16_bf16 v[68:83], a[220:223], a[44:47], v[68:83]\n"
    "s_xor_b64 s[92:93], s[60:61], s[56:57]\n"
    "s_xor_b64 vcc, s[62:63], s[56:57]\n"
    "s_or_b64 s[92:93], s[92:93], vcc\n"
    "s_cbranch_scc0 .Lhh_s8d\n"
    "s_mov_b32 s58, 48\n"
    ".Lhh_s8a:\n"
    "s_buffer_load_dwordx4 s[60:63], s[16:19], s78 glc\n"
    "s_sleep 1\n"
    "s_xor_b64 s[92:93], s[60:61], s[56:57]\n"
    "s_xor_b64 vcc, s[62:63], s[56:57]\n"
    "s_or_b64 s[92:93], s[92:93], vcc\n"
    "s_cbranch_scc0 .Lhh_s8d\n"
    "s_sleep 1\n"
    "s_xor_b64 s[92:93], s[60:61], s[56:57]\n"
    "s_xor_b64 vcc, s[62:63], s[56:57]\n"
    "s_or_b64 s[92:93], s[92:93], vcc\n"
    "s_cbranch_scc0 .Lhh_s8d\n"
    "s_sleep 1\n"
    "s_xor_b64 s[92:93], s[60:61], s[56:57]\n"
    "s_xor_b64 vcc, s[62:63], s[56:57]\n"
    "s_or_b64 s[92:93], s[92:93], vcc\n"
    "s_cbranch_scc0 .Lhh_s8d\n"
    "s_sub_u32 s58, s58, 1\n"
    "s_cbranch_scc0 .Lhh_s8a\n"
    "s_branch .Lhh_s8\n"
    ".Lhh_s8d:\n"
    "s_waitcnt vmcnt(9)\n"
    "v_mfma_f32_32x32x16_bf16 v[52:67], a[224:227], a[48:51], v[52:67]\n"
    "s_add_u32 s82, s54, 0x16000\n"
    "buffer_load_dwordx4 a[88:91], v14, s[16:19], s82 offen sc1\n"
    "v_mfma_f32_32x32x16_bf16 v[68:83], a[228:231], a[48:51], v[68:83]\n"
    "s_waitcnt vmcnt(9)\n"
    "v_mfma_f32_32x32x16_bf16 v[52:67], a[232:235], a[52:55], v[52:67]\n"
    "buffer_load_dwordx4 a[92:95], v14, s[16:19], s82 offen offset:1024 sc1\n"
    "v_mfma_f32_32x32x16_bf16 v[68:83], a[236:239], a[52:55], v[68:83]\n"
    "s_waitcnt vmcnt(9)\n"
    "v_mfma_f32_32x32x16_bf16 v[52:67], a[240:243], a[56:59], v[52:67]\n"
    "buffer_load_dwordx4 a[96:99], v14, s[16:19], s82 offen offset:2048 sc1\n"
    "v_mfma_f32_32x32x16_bf16 v[68:83], a[244:247], a[56:59], v[68:83]\n"
    "s_waitcnt vmcnt(9)\n"
    "v_mfma_f32_32x32x16_bf16 v[52:67], a[248:251], a[60:63], v[52:67]\n"
    "buffer_load_dwordx4 a[100:103], v14, s[16:19], s82 offen offset:3072 sc1\n"
    "v_mfma_f32_32x32x16_bf16 v[68:83], a[252:255], a[60:63], v[68:83]\n"
    "s_waitcnt vmcnt(9)\n"
    "v_mfma_f32_32x32x16_bf16 v[52:67], v[144:147], a[64:67], v[52:67]\n"
    "s_add_u32 s82, s54, 0x17000\n"
    "buffer_load_dwordx4 a[104:107], v14, s[16:19], s82 offen sc1\n"
    "v_mfma_f32_32x32x16_bf16 v[68:83], v[148:151], a[64:67], v[68:83]\n"
    "s_waitcnt vmcnt(9)\n"
    "v_mfma_f32_32x32x16_bf16 v[52:67], v[152:155], a[68:71], v[52:67]\n"
    "buffer_load_dwordx4 a[108:111], v14, s[16:19], s82 offen offset:1024 sc1\n"
    "v_mfma_f32_32x32x16_bf16 v[68:83], v[156:159], a[68:71], v[68:83]\n"
    "s_waitcnt vmcnt(9)\n"
    "v_mfma_f32_32x32x16_bf16 v[52:67], v[160:163], a[72:75], v[52:67]\n"
    "buffer_load_dwordx4 a[112:115], v14, s[16:19], s82 offen offset:2048 sc1\n"
    "v_mfma_f32_32x32x16_bf16 v[68:83], v[164:167], a[72:75], v[68:83]\n"
    "s_waitcnt vmcnt(9)\n"
    "v_mfma_f32_32x32x16_bf16 v[52:67], v[168:171], a[76:79], v[52:67]\n"
    "buffer_load_dwordx4 a[116:119], v14, s[16:19], s82 offen offset:3072 sc1\n"
    "v_mfma_f32_32x32x16_bf16 v[68:83], v[172:175], a[76:79], v[68:83]\n"
    "s_waitcnt vmcnt(9)\n"
    "v_mfma_f32_32x32x16_bf16 v[52:67], v[176:179], a[80:83], v[52:67]\n"
    "s_add_u32 s82, s54, 0x18000\n"
    "buffer_load_dwordx4 a[120:123], v14, s[16:19], s82 offen sc1\n"
    "v_mfma_f32_32x32x16_bf16 v[68:83], v[180:183], a[80:83], v[68:83]\n"
    "s_waitcnt vmcnt(9)\n"
    "v_mfma_f32_32x32x16_bf16 v[52:67], v[184:187], a[84:87], v[52:67]\n"
    "buffer_load_dwordx4 a[124:127], v14, s[16:19], s82 offen offset:1024 sc1\n"
    "v_mfma_f32_32x32x16_bf16 v[68:83], v[188:191], a[84:87], v[68:83]\n"
    "s_waitcnt vmcnt(9)\n"
    "v_mfma_f32_32x32x16_bf16 v[84:99], v[192:195], a[88:91], 0\n"
    "buffer_load_dwordx4 a[128:131], v14, s[16:19], s82 offen offset:2048 sc1\n"
    "v_mfma_f32_32x32x16_bf16 v[100:115], v[196:199], a[88:91], 0\n"
    "s_waitcnt vmcnt(9)\n"
    "v_mfma_f32_32x32x16_bf16 v[84:99], v[200:203], a[92:95], v[84:99]\n"
    "buffer_load_dwordx4 a[132:135], v14, s[16:19], s82 offen offset:3072 sc1\n"
    "v_mfma_f32_32x32x16_bf16 v[100:115], v[204:207], a[92:95], v[100:115]\n"
    "s_waitcnt vmcnt(9)\n"
    "v_mfma_f32_32x32x16_bf16 v[84:99], v[208:211], a[96:99], v[84:99]\n"
    "s_add_u32 s82, s54, 0x19000\n"
    "buffer_load_dwordx4 a[136:139], v14, s[16:19], s82 offen sc1\n"
    "ds_write_b128 v19, v[52:55]\n"
    "v_mfma_f32_32x32x16_bf16 v[100:115], v[212:215], a[96:99], v[100:115]\n"
    "ds_write_b128 v19, v[56:59] offset:1152\n"
    "ds_write_b128 v19, v[60:63] offset:2304\n"
    "s_waitcnt vmcnt(9)\n"
    "v_mfma_f32_32x32x16_bf16 v[84:99], v[216:219], a[100:103], v[84:99]\n"
    "buffer_load_dwordx4 a[140:143], v14, s[16:19], s82 offen offset:1024 sc1\n"
    "ds_write_b128 v19, v[64:67] offset:3456\n"
    "ds_write_b128 v19, v[68:71] offset:4608\n"
    "v_mfma_f32_32x32x16_bf16 v[100:115], v[220:223], a[100:103], v[100:115]\n"
    "ds_write_b128 v19, v[72:75] offset:5760\n"
    "ds_write_b128 v19, v[76:79] offset:6912\n"
    "s_waitcnt vmcnt(9)\n"
    "v_mfma_f32_32x32x16_bf16 v[84:99], v[224:227], a[104:107], v[84:99]\n"
    "buffer_load_dwordx4 a[144:147], v14, s[16:19], s82 offen offset:2048 sc1\n"
    "ds_write_b128 v19, v[80:83] offset:8064\n"
    "v_mfma_f32_32x32x16_bf16 v[100:115], v[228:231], a[104:107], v[100:115]\n"
    "s_waitcnt vmcnt(9)\n"
    "v_mfma_f32_32x32x16_bf16 v[84:99], v[232:235], a[108:111], v[84:99]\n"
    "buffer_load_dwordx4 a[148:151], v14, s[16:19], s82 offen offset:3072 sc1\n"
    "v_mfma_f32_32x32x16_bf16 v[100:115], v[236:239], a[108:111], v[100:115]\n"
    "s_waitcnt vmcnt(9)\n"
    "v_mfma_f32_32x32x16_bf16 v[84:99], v[240:243], a[112:115], v[84:99]\n"
    "s_add_u32 s82, s54, 0x1a000\n"
    "buffer_load_dwordx4 a[152:155], v14, s[16:19], s82 offen sc1\n"
    "v_mfma_f32_32x32x16_bf16 v[100:115], v[244:247], a[112:115], v[100:115]\n"
    "s_waitcnt vmcnt(9)\n"
    "v_mfma_f32_32x32x16_bf16 v[84:99], v[248:251], a[116:119], v[84:99]\n"
    "buffer_load_dwordx4 a[156:159], v14, s[16:19], s82 offen offset:1024 sc1\n"
    "v_mfma_f32_32x32x16_bf16 v[100:115], v[252:255], a[116:119], v[100:115]\n"
    "s_waitcnt lgkmcnt(0)\n"
    "s_barrier\n"
    "s_waitcnt vmcnt(9)\n"
    "v_mfma_f32_32x32x16_bf16 v[84:99], a[192:195], a[120:123], v[84:99]\n"
    "buffer_load_dwordx4 a[160:163], v14, s[16:19], s82 offen offset:2048 sc1\n"
    "ds_read_b128 v[52:55], v28\n"
    "ds_read_b128 v[56:59], v28 offset:512\n"
    "ds_read_b128 v[60:63], v28 offset:18432\n"
    "ds_read_b128 v[64:67], v28 offset:18944\n"
    "ds_read_b128 v[68:71], v28 offset:36864\n"
    "v_mfma_f32_32x32x16_bf16 v[100:115], a[196:199], a[120:123], v[100:115]\n"
    "ds_read_b128 v[72:75], v28 offset:37376\n"
    "ds_read_b128 v[76:79], v28 offset:55296\n"
    "ds_read_b128 v[80:83], v28 offset:55808\n"
    "s_waitcnt vmcnt(9)\n"
    "v_mfma_f32_32x32x16_bf16 v[84:99], a[200:203], a[124:127], v[84:99]\n"
    "buffer_load_dwordx4 a[164:167], v14, s[16:19], s82 offen offset:3072 sc1\n"
    "v_mfma_f32_32x32x16_bf16 v[100:115], a[204:207], a[124:127], v[100:115]\n"
    "s_waitcnt vmcnt(9)\n"
    "v_mfma_f32_32x32x16_bf16 v[84:99], a[208:211], a[128:131], v[84:99]\n"
    "s_add_u32 s82, s54, 0x1b000\n"
    "buffer_load_dwordx4 a[168:171], v17, s[16:19], s82 offen sc1\n"
    "v_mfma_f32_32x32x16_bf16 v[100:115], a[212:215], a[128:131], v[100:115]\n"
    "s_waitcnt vmcnt(9)\n"
    "v_mfma_f32_32x32x16_bf16 v[84:99], a[216:219], a[132:135], v[84:99]\n"
    "buffer_load_dwordx4 a[172:175], v17, s[16:19], s82 offen offset:1024 sc1\n"
    "s_waitcnt lgkmcnt(7)\n"
    "v_add_f32 v120, v120, v52\n"
    "v_add_f32 v121, v121, v53\n"
    "v_add_f32 v122, v122, v54\n"
    "v_add_f32 v123, v123, v55\n"
    "s_waitcnt lgkmcnt(6)\n"
    "v_add_f32 v124, v124, v56\n"
    "v_mfma_f32_32x32x16_bf16 v[100:115], a[220:223], a[132:135], v[100:115]\n"
    "v_add_f32 v125, v125, v57\n"
    "v_add_f32 v126, v126, v58\n"
    "v_add_f32 v127, v127, v59\n"
    "s_waitcnt lgkmcnt(5)\n"
    "v_add_f32 v120, v120, v60\n"
    "v_add_f32 v121, v121, v61\n"
    "v_add_f32 v122, v122, v62\n"
    "s_waitcnt vmcnt(9)\n"
    "v_mfma_f32_32x32x16_bf16 v[84:99], a[224:227], a[136:139], v[84:99]\n"
    "v_add_f32 v123, v123, v63\n"
    "s_waitcnt lgkmcnt(4)\n"
    "v_add_f32 v124, v124, v64\n"
    "v_add_f32 v125, v125, v65\n"
    "v_add_f32 v126, v126, v66\n"
    "v_add_f32 v127, v127, v67\n"
    "s_waitcnt lgkmcnt(3)\n"
    "v_add_f32 v120, v120, v68\n"
    "v_mfma_f32_32x32x16_bf16 v[100:115], a[228:231], a[136:139], v[100:115]\n"
    "v_add_f32 v121, v121, v69\n"
    "v_add_f32 v122, v122, v70\n"
    "v_add_f32 v123, v123, v71\n"
    "s_waitcnt lgkmcnt(2)\n"
    "v_add_f32 v124, v124, v72\n"
    "v_add_f32 v125, v125, v73\n"
    "v_add_f32 v126, v126, v74\n"
    "s_waitcnt vmcnt(8)\n"
    "v_mfma_f32_32x32x16_bf16 v[84:99], a[232:235], a[140:143], v[84:99]\n"
    "v_add_f32 v127, v127, v75\n"
    "s_waitcnt lgkmcnt(1)\n"
    "v_add_f32 v120, v120, v76\n"
    "v_add_f32 v121, v121, v77\n"
    "v_add_f32 v122, v122, v78\n"
    "v_add_f32 v123, v123, v79\n"
    "s_waitcnt lgkmcnt(0)\n"
    "v_add_f32 v124, v124, v80\n"
    "v_mfma_f32_32x32x16_bf16 v[100:115], a[236:239], a[140:143], v[100:115]\n"
    "v_add_f32 v125, v125, v81\n"
    "v_add_f32 v126, v126, v82\n"
    "v_add_f32 v127, v127, v83\n"
    "v_cvt_pk_bf16_f32 v136, v120, v121\n"
    "v_cvt_pk_bf16_f32 v137, v122, v123\n"
    "v_cvt_pk_bf16_f32 v138, v124, v125\n"
    "s_waitcnt vmcnt(7)\n"
    "v_mfma_f32_32x32x16_bf16 v[84:99], a[240:243], a[144:147], v[84:99]\n"
    "v_cvt_pk_bf16_f32 v139, v126, v127\n"
    "v_mfma_f32_32x32x16_bf16 v[100:115], a[244:247], a[144:147], v[100:115]\n"
    "buffer_store_dwordx4 v[136:139], v7, s[44:47], s53 offen\n"
    "s_waitcnt vmcnt(7)\n"
    "v_mfma_f32_32x32x16_bf16 v[84:99], a[248:251], a[148:151], v[84:99]\n"
    "v_mfma_f32_32x32x16_bf16 v[100:115], a[252:255], a[148:151], v[100:115]\n"
    "s_waitcnt vmcnt(6)\n"
    "v_mfma_f32_32x32x16_bf16 v[84:99], v[144:147], a[152:155], v[84:99]\n"
    "v_mfma_f32_32x32x16_bf16 v[100:115], v[148:151], a[152:155], v[100:115]\n"
    "s_waitcnt vmcnt(5)\n"
    "v_mfma_f32_32x32x16_bf16 v[84:99], v[152:155], a[156:159], v[84:99]\n"
    "v_mfma_f32_32x32x16_bf16 v[100:115], v[156:159], a[156:159], v[100:115]\n"
    "s_waitcnt vmcnt(4)\n"
    "v_mfma_f32_32x32x16_bf16 v[84:99], v[160:163], a[160:163], v[84:99]\n"
    "v_mfma_f32_32x32x16_bf16 v[100:115], v[164:167], a[160:163], v[100:115]\n"
    "s_waitcnt vmcnt(3)\n"
    "v_mfma_f32_32x32x16_bf16 v[84:99], v[168:171], a[164:167], v[84:99]\n"
    "v_mfma_f32_32x32x16_bf16 v[100:115], v[172:175], a[164:167], v[100:115]\n"
    "s_waitcnt vmcnt(2)\n"
    "v_mfma_f32_32x32x16_bf16 v[84:99], v[176:179], a[168:171], v[84:99]\n"
    "v_mfma_f32_32x32x16_bf16 v[100:115], v[180:183], a[168:171], v[100:115]\n"
    "s_waitcnt vmcnt(1)\n"
    "v_mfma_f32_32x32x16_bf16 v[84:99], v[184:187], a[172:175], v[84:99]\n"
    "v_mfma_f32_32x32x16_bf16 v[100:115], v[188:191], a[172:175], v[100:115]\n"
    "s_nop 10\n"
    "ds_write_b128 v19, v[84:87] offset:9216\n"
    "ds_write_b128 v19, v[88:91] offset:10368\n"
    "ds_write_b128 v19, v[92:95] offset:11520\n"
    "ds_write_b128 v19, v[96:99] offset:12672\n"
    "ds_write_b128 v19, v[100:103] offset:13824\n"
    "ds_write_b128 v19, v[104:107] offset:14976\n"
    "ds_write_b128 v19, v[108:111] offset:16128\n"
    "ds_write_b128 v19, v[112:115] offset:17280\n"
    "s_waitcnt lgkmcnt(0)\n"
    "s_barrier\n"
    "ds_read_b128 v[84:87], v28 offset:9216\n"
    "ds_read_b128 v[88:91], v28 offset:9728\n"
    "ds_read_b128 v[92:95], v28 offset:27648\n"
    "ds_read_b128 v[96:99], v28 offset:28160\n"
    "ds_read_b128 v[100:103], v28 offset:46080\n"
    "ds_read_b128 v[104:107], v28 offset:46592\n"
    "ds_read_b128 v[108:111], v28 offset:64512\n"
    "ds_read_b128 v[112:115], v28 offset:65024\n"
    "s_waitcnt lgkmcnt(7)\n"
    "v_pk_add_f32 v[128:129], v[128:129], v[84:85]\n"
    "v_pk_add_f32 v[130:131], v[130:131], v[86:87]\n"
    "s_waitcnt lgkmcnt(6)\n"
    "v_pk_add_f32 v[132:133], v[132:133], v[88:89]\n"
    "v_pk_add_f32 v[134:135], v[134:135], v[90:91]\n"
    "s_waitcnt lgkmcnt(5)\n"
    "v_pk_add_f32 v[128:129], v[128:129], v[92:93]\n"
    "v_pk_add_f32 v[130:131], v[130:131], v[94:95]\n"
    "s_waitcnt lgkmcnt(4)\n"
    "v_pk_add_f32 v[132:133], v[132:133], v[96:97]\n"
    "v_pk_add_f32 v[134:135], v[134:135], v[98:99]\n"
    "s_waitcnt lgkmcnt(3)\n"
    "v_pk_add_f32 v[128:129], v[128:129], v[100:101]\n"
    "v_pk_add_f32 v[130:131], v[130:131], v[102:103]\n"
    "s_waitcnt lgkmcnt(2)\n"
    "v_pk_add_f32 v[132:133], v[132:133], v[104:105]\n"
    "v_pk_add_f32 v[134:135], v[134:135], v[106:107]\n"
    "s_waitcnt lgkmcnt(1)\n"
    "v_pk_add_f32 v[128:129], v[128:129], v[108:109]\n"
    "v_pk_add_f32 v[130:131], v[130:131], v[110:111]\n"
    "s_waitcnt lgkmcnt(0)\n"
    "v_pk_add_f32 v[132:133], v[132:133], v[112:113]\n"
    "v_pk_add_f32 v[134:135], v[134:135], v[114:115]\n"
    "v_cvt_pk_bf16_f32 v140, v128, v129\n"
    "v_cvt_pk_bf16_f32 v141, v130, v131\n"
    "v_cvt_pk_bf16_f32 v142, v132, v133\n"
    "v_cvt_pk_bf16_f32 v143, v134, v135\n"
    "buffer_store_dwordx4 v[140:143], v7, s[44:47], s55 offen\n"
    "s_endpgm\n"
    ".Lhh_p1:\n"
    "s_mov_b32 s58, 0x10000\n"
    ".Lhh_p1l:\n"
    "s_sleep 1\n"
    "buffer_load_dwordx2 v[10:11], v19, s[16:19], s86 offen sc1\n"
    "s_waitcnt vmcnt(0)\n"
    "v_cmp_ne_u64 vcc, s[56:57], v[10:11]\n"
    "s_sub_u32 s58, s58, 1\n"
    "s_cbranch_scc1 .Lhh_p1d\n"
    "s_cbranch_vccnz .Lhh_p1l\n"
    "s_branch .Lhh_p1d\n"
    ".Lhh_v2:\n"
    "s_mov_b32 s58, 0x10000\n"
    ".Lhh_v2l:\n"
    "buffer_load_dwordx4 v[130:133], v5, s[16:19], s55 offen sc1\n"
    "buffer_load_dwordx4 v[134:137], v6, s[16:19], s55 offen offset:1024 sc1\n"
    "s_waitcnt vmcnt(1)\n"
    "v_max3_u32 v10, v130, v131, v132\n"
    "s_waitcnt vmcnt(0)\n"
    "v_max3_u32 v11, v133, v134, v135\n"
    "v_max3_u32 v10, v10, v136, v137\n"
    "v_max_u32 v10, v10, v11\n"
    "v_cmp_eq_u32 vcc, -1, v10\n"
    "s_sub_u32 s58, s58, 1\n"
    "s_cbranch_scc1 .Lhh_v2d\n"
    "s_cbranch_vccnz .Lhh_v2l\n"
    "s_branch .Lhh_v2d\n"
    ".Lhh_v3:\n"
    "s_mov_b32 s58, 0x10000\n"
    ".Lhh_v3l:\n"
    "buffer_load_dwordx4 v[130:133], v14, s[16:19], s54 offen sc1\n"
    "buffer_load_dwordx4 v[134:137], v6, s[16:19], s54 offen offset:1024 sc1\n"
    "s_waitcnt vmcnt(1)\n"
    "v_max3_u32 v10, v130, v131, v132\n"
    "s_waitcnt vmcnt(0)\n"
    "v_max3_u32 v11, v133, v134, v135\n"
    "v_max3_u32 v10, v10, v136, v137\n"
    "v_max_u32 v10, v10, v11\n"
    "v_cmp_eq_u32 vcc, -1, v10\n"
    "s_sub_u32 s58, s58, 1\n"
    "s_cbranch_scc1 .Lhh_v3d\n"
    "s_cbranch_vccnz .Lhh_v3l\n"
    "s_branch .Lhh_v3d\n"
    ".Lhh_s4:\n"
    "s_mov_b32 s58, 0x10000\n"
    ".Lhh_s4l:\n"
    "s_sleep 2\n"
    "buffer_load_dwordx2 v[10:11], v15, s[16:19], s78 offen sc1\n"
    "s_waitcnt vmcnt(0)\n"
    "v_cmp_ne_u64 vcc, s[56:57], v[10:11]\n"
    "s_sub_u32 s58, s58, 1\n"
    "s_cbranch_scc1 .Lhh_s4d\n"
    "s_cbranch_vccnz .Lhh_s4l\n"
    "s_branch .Lhh_s4d\n"
    ".Lhh_s5:\n"
    "s_mov_b32 s58, 0x10000\n"
    ".Lhh_s5l:\n"
    "s_sleep 2\n"
    "buffer_load_dwordx2 v[10:11], v15, s[16:19], s78 offen sc1\n"
    "s_waitcnt vmcnt(0)\n"
    "v_cmp_ne_u64 vcc, s[56:57], v[10:11]\n"
    "s_sub_u32 s58, s58, 1\n"
    "s_cbranch_scc1 .Lhh_s5d\n"
    "s_cbranch_vccnz .Lhh_s5l\n"
    "s_branch .Lhh_s5d\n"
    ".Lhh_p6:\n"
    "s_mov_b32 s58, 0x10000\n"
    ".Lhh_p6l:\n"
    "s_sleep 1\n"
    "buffer_load_dwordx2 v[10:11], v15, s[16:19], s78 offen sc1\n"
    "s_waitcnt vmcnt(0)\n"
    "v_cmp_ne_u64 vcc, s[56:57], v[10:11]\n"
    "s_sub_u32 s58, s58, 1\n"
    "s_cbranch_scc1 .Lhh_p6d\n"
    "s_cbranch_vccnz .Lhh_p6l\n"
    "s_branch .Lhh_p6d\n"
    ".Lhh_s7:\n"
    "s_mov_b32 s58, 0x10000\n"
    ".Lhh_s7l:\n"
    "s_sleep 2\n"
    "buffer_load_dwordx2 v[10:11], v15, s[16:19], s78 offen sc1\n"
    "s_waitcnt vmcnt(0)\n"
    "v_cmp_ne_u64 vcc, s[56:57], v[10:11]\n"
    "s_sub_u32 s58, s58, 1\n"
    "s_cbranch_scc1 .Lhh_s7d\n"
    "s_cbranch_vccnz .Lhh_s7l\n"
    "s_branch .Lhh_s7d\n"
    ".Lhh_s8:\n"
    "s_mov_b32 s58, 0x10000\n"
    ".Lhh_s8l:\n"
    "s_sleep 2\n"
    "buffer_load_dwordx2 v[10:11], v15, s[16:19], s78 offen sc1\n"
    "s_waitcnt vmcnt(0)\n"
    "v_cmp_ne_u64 vcc, s[56:57], v[10:11]\n"
    "s_sub_u32 s58, s58, 1\n"
    "s_cbranch_scc1 .Lhh_s8d\n"
    "s_cbranch_vccnz .Lhh_s8l\n"
    "s_branch .Lhh_s8d\n"
    ".Lhh_end:\n"
    : "+v"(tid)
    : "s"(__builtin_amdgcn_kernarg_segment_ptr()), "s"(blockIdx.x)
    : "v1", "v2", "v3", "v4", "v5", "v6", "v7", "v8", "v9", "v10", "v11", "v12", "v13", "v14", "v15", "v16", "v17", "v18", "v19", "v20", "v21", "v22", "v23", "v24", "v25", "v26", "v27", "v28", "v29", "v30", "v31", "v32", "v33", "v34", "v35", "v36", "v37", "v38", "v39", "v40", "v41", "v42", "v43", "v44", "v45", "v46", "v47", "v48", "v49", "v50", "v51", "v52", "v53", "v54", "v55", "v56", "v57", "v58", "v59", "v60", "v61", "v62", "v63", "v64", "v65", "v66", "v67", "v68", "v69", "v70", "v71", "v72", "v73", "v74", "v75", "v76", "v77", "v78", "v79", "v80", "v81", "v82", "v83", "v84", "v85", "v86", "v87", "v88", "v89", "v90", "v91", "v92", "v93", "v94", "v95", "v96", "v97", "v98", "v99", "v100", "v101", "v102", "v103", "v104", "v105", "v106", "v107", "v108", "v109", "v110", "v111", "v112", "v113", "v114", "v115", "v116", "v117", "v118", "v119", "v120", "v121", "v122", "v123", "v124", "v125", "v126", "v127", "v128", "v129", "v130", "v131", "v132", "v133", "v134", "v135", "v136", "v137", "v138", "v139", "v140", "v141", "v142", "v143", "v144", "v145", "v146", "v147", "v148", "v149", "v150", "v151", "v152", "v153", "v154", "v155", "v156", "v157", "v158", "v159", "v160", "v161", "v162", "v163", "v164", "v165", "v166", "v167", "v168", "v169", "v170", "v171", "v172", "v173", "v174", "v175", "v176", "v177", "v178", "v179", "v180", "v181", "v182", "v183", "v184", "v185", "v186", "v187", "v188", "v189", "v190", "v191", "v192", "v193", "v194", "v195", "v196", "v197", "v198", "v199", "v200", "v201", "v202", "v203", "v204", "v205", "v206", "v207", "v208", "v209", "v210", "v211", "v212", "v213", "v214", "v215", "v216", "v217", "v218", "v219", "v220", "v221", "v222", "v223", "v224", "v225", "v226", "v227", "v228", "v229", "v230", "v231", "v232", "v233", "v234", "v235", "v236", "v237", "v238", "v239", "v240", "v241", "v242", "v243", "v244", "v245", "v246", "v247", "v248", "v249", "v250", "v251", "v252", "v253", "v254", "v255", "a0", "a1", "a2", "a3", "a4", "a5", "a6", "a7", "a8", "a9", "a10", "a11", "a12", "a13", "a14", "a15", "a16", "a17", "a18", "a19", "a20", "a21", "a22", "a23", "a24", "a25", "a26", "a27", "a28", "a29", "a30", "a31", "a32", "a33", "a34", "a35", "a36", "a37", "a38", "a39", "a40", "a41", "a42", "a43", "a44", "a45", "a46", "a47", "a48", "a49", "a50", "a51", "a52", "a53", "a54", "a55", "a56", "a57", "a58", "a59", "a60", "a61", "a62", "a63", "a64", "a65", "a66", "a67", "a68", "a69", "a70", "a71", "a72", "a73", "a74", "a75", "a76", "a77", "a78", "a79", "a80", "a81", "a82", "a83", "a84", "a85", "a86", "a87", "a88", "a89", "a90", "a91", "a92", "a93", "a94", "a95", "a96", "a97", "a98", "a99", "a100", "a101", "a102", "a103", "a104", "a105", "a106", "a107", "a108", "a109", "a110", "a111", "a112", "a113", "a114", "a115", "a116", "a117", "a118", "a119", "a120", "a121", "a122", "a123", "a124", "a125", "a126", "a127", "a128", "a129", "a130", "a131", "a132", "a133", "a134", "a135", "a136", "a137", "a138", "a139", "a140", "a141", "a142", "a143", "a144", "a145", "a146", "a147", "a148", "a149", "a150", "a151", "a152", "a153", "a154", "a155", "a156", "a157", "a158", "a159", "a160", "a161", "a162", "a163", "a164", "a165", "a166", "a167", "a168", "a169", "a170", "a171", "a172", "a173", "a174", "a175", "a176", "a177", "a178", "a179", "a180", "a181", "a182", "a183", "a184", "a185", "a186", "a187", "a188", "a189", "a190", "a191", "a192", "a193", "a194", "a195", "a196", "a197", "a198", "a199", "a200", "a201", "a202", "a203", "a204", "a205", "a206", "a207", "a208", "a209", "a210", "a211", "a212", "a213", "a214", "a215", "a216", "a217", "a218", "a219", "a220", "a221", "a222", "a223", "a224", "a225", "a226", "a227", "a228", "a229", "a230", "a231", "a232", "a233", "a234", "a235", "a236", "a237", "a238", "a239", "a240", "a241", "a242", "a243", "a244", "a245", "a246", "a247", "a248", "a249", "a250", "a251", "a252", "a253", "a254", "a255", "s8", "s9", "s10", "s11", "s12", "s13", "s14", "s15", "s16", "s17", "s18", "s19", "s20", "s21", "s22", "s23", "s24", "s25", "s26", "s27", "s28", "s29", "s30", "s31", "s33", "s34", "s35", "s36", "s37", "s38", "s39", "s40", "s41", "s42", "s43", "s44", "s45", "s46", "s47", "s48", "s49", "s50", "s51", "s52", "s53", "s54", "s55", "s56", "s57", "s58", "s59", "s60", "s61", "s62", "s63", "s64", "s65", "s66", "s67", "s68", "s69", "s70", "s71", "s72", "s73", "s74", "s75", "s76", "s77", "s78", "s79", "s80", "s81", "s82", "s83", "s84", "s85", "s86", "s87", "s88", "s89", "s90", "s91", "s92", "s93", "s94", "s95", "s96", "s97", "s98", "s99", "vcc", "scc", "memory");
}

extern "C" void kernel_launch(void* const* d_in, const int* in_sizes, int n_in, void* d_out, int out_size, void* d_ws, size_t ws_size, hipStream_t stream) {
  (void)hipFuncSetAttribute((const void*)hh_block, hipFuncAttributeMaxDynamicSharedMemorySize, 163840);
  hh_block<<<256, 256, 163840, stream>>>(d_in[0], 2097152u, 0x00020000u, d_in[1], 1572864u, 0x00020000u, d_ws, 15605760u, 0x00020000u, d_in[2], 16384u, 0x00020000u, d_in[3], 524288u, 0x00020000u, d_in[7], 1409024u, 0x00020000u, d_in[5], 1409024u, 0x00020000u, d_in[6], 2752u, 0x00020000u, d_in[8], 1024u, 0x00020000u, d_out, 2097152u, 0x00020000u);
}
```
